```python
import torch

def _softmax(x, axis=-1):
    return torch.softmax(x, dim=axis)
def _gelu(x):
    return torch.nn.functional.gelu(x, approximate="tanh")
def _silu(x):
    return torch.nn.functional.silu(x)
def _sigmoid(x):
    return torch.sigmoid(x)
def _layernorm(x, axis=-1, eps=1e-5):
    m = x.mean(dim=axis, keepdim=True)
    v = x.var(dim=axis, keepdim=True, unbiased=False)
    return (x - m) / torch.sqrt(v + eps)
def _scatter_add(tgt, idx, src, axis=0):
    return tgt.clone().index_add_(axis, idx, src)
def _segment_sum(src, seg, n):
    out = torch.zeros((n,) + src.shape[1:], dtype=src.dtype)
    return out.index_add_(0, seg, src)
def _pad(x, pw):
    flat = tuple(v for pair in reversed(pw) for v in pair)
    return torch.nn.functional.pad(x, flat)

kernel_name = 'standalone__spectral_conv_chebnet__graph'

def setup_inputs():

    _w = (lambda a: (lambda t: t.to(torch.bfloat16) if t.is_floating_point() else t)(torch.as_tensor(a)))
    N, deg, D, K = 50000, 16, 64, 2
    E = N * deg
    src = (lambda lo, hi, *s: torch.randint(lo, hi, s))(0, N, E).to(torch.int64)
    dst = (lambda lo, hi, *s: torch.randint(lo, hi, s))(0, N, E).to(torch.int64)
    deg_cnt = torch.bincount(dst, minlength=N).to(float) + 1.0
    dis = (1.0 / (lambda _x: torch.sqrt(torch.as_tensor(_x)))(deg_cnt)).to(float)
    ew = -(dis[src] * dis[dst])
    return {
        "x": _w((lambda *s: torch.randn(*s, dtype=torch.bfloat16) * 0.05)(N, D)),
        "src": _w(src),
        "dst": _w(dst),
        "ew": _w(ew),
        "w": _w((lambda *s: torch.randn(*s, dtype=torch.bfloat16) * 0.05)(K, D, D) * 0.2),
    }

def reference(x, src, dst, ew, w):
    N, K = x.shape[0], w.shape[0]
    Ts = [x]
    if K > 1:
        Ts.append(_segment_sum(ew[:, None] * x[src], dst, N))
    for k in range(2, K):
        Tk = 2.0 * _segment_sum(ew[:, None] * Ts[-1][src], dst, N) - Ts[-2]
        Ts.append(Tk)
    T = torch.stack(Ts, 0)
    return torch.einsum('knd,kde->ne', T, w)
```

```cpp
#include <hip/hip_runtime.h>
#include <hip/hip_bf16.h>
#include <stdint.h>

typedef __attribute__((ext_vector_type(4))) short s16x4;
typedef __attribute__((ext_vector_type(8))) short bf16x8;
typedef __attribute__((ext_vector_type(4))) float f32x4;
typedef __attribute__((ext_vector_type(2))) float f32x2;
typedef __attribute__((ext_vector_type(2))) __bf16 bf16x2v;
typedef __attribute__((ext_vector_type(4))) unsigned u32x4;
typedef __attribute__((ext_vector_type(2))) unsigned u32x2;
typedef __attribute__((ext_vector_type(4))) int i32x4;
typedef __attribute__((ext_vector_type(4))) unsigned short u16x4;
#define LAS __attribute__((address_space(3)))

constexpr int DF = 64;
constexpr int K1_THREADS = 1024;
constexpr int MAX_BINS = 4096;
constexpr int MAX_CH = 4096;
constexpr int K2_WAVES = 13;
constexpr int K2_GRID = 256;
constexpr int CAP = 512;
constexpr int WAVE_LDS = 8192 + 8 * CAP;

__device__ __forceinline__ unsigned pk_bf16(float a, float b) {
  f32x2 v = {a, b};
  bf16x2v r = __builtin_convertvector(v, bf16x2v);
  return __builtin_bit_cast(unsigned, r);
}

__device__ __forceinline__ unsigned wave_incl_scan(unsigned v, int lane) {
#pragma unroll
  for (int d = 1; d < 64; d <<= 1) {
    unsigned o = __shfl_up(v, d, 64);
    if (lane >= d) v += o;
  }
  return v;
}

__global__ __launch_bounds__(K1_THREADS) void k_part(
    const int* __restrict__ src, const int* __restrict__ dst, const uint16_t* __restrict__ ew,
    const uint16_t* __restrict__ w, u32x2* __restrict__ recs, uint16_t* __restrict__ tab,
    uint16_t* __restrict__ wfrag, int E, int CH, int P, int pitch) {
  __shared__ __attribute__((aligned(16))) unsigned hist[MAX_BINS];
  __shared__ __attribute__((aligned(16))) u32x2 lrec[MAX_CH];
  __shared__ unsigned wsum[16];
  const int b = blockIdx.x, t = threadIdx.x, lane = t & 63, wv = t >> 6;
  const int e0 = b * CH;
  const int cnt = min(CH, E - e0);
  const int i0 = 4 * t;
  int d[4], s[4];
  unsigned wt[4];
  if (i0 + 4 <= cnt) {
    i32x4 d4 = *(const i32x4*)(dst + e0 + i0);
    i32x4 s4 = *(const i32x4*)(src + e0 + i0);
    u16x4 w4 = *(const u16x4*)(ew + e0 + i0);
#pragma unroll
    for (int j = 0; j < 4; ++j) { d[j] = d4[j]; s[j] = s4[j]; wt[j] = w4[j]; }
  } else {
#pragma unroll
    for (int j = 0; j < 4; ++j) {
      bool ok = i0 + j < cnt;
      d[j] = ok ? dst[e0 + i0 + j] : -1;
      s[j] = ok ? src[e0 + i0 + j] : 0;
      wt[j] = ok ? ew[e0 + i0 + j] : 0;
    }
  }
  ((u32x4*)hist)[t] = u32x4{0u, 0u, 0u, 0u};
  if (wv == 15) {
    for (int f = b; f < 16; f += gridDim.x) {
      const int kk = f >> 2, u = f & 3, i = lane & 15, q = lane >> 4;
      const int e = 32 * (u >> 1) + 8 * (i >> 2) + 4 * (u & 1) + (i & 3);
      unsigned short v[8];
#pragma unroll
      for (int j = 0; j < 8; ++j) {
        int dd, kw;
        if (kk < 2) { kw = 0; dd = 32 * kk + 8 * q + j; }
        else { kw = 1; dd = 16 * (2 * (kk - 2) + (j >> 2)) + 4 * q + (j & 3); }
        v[j] = w[(kw * DF + dd) * DF + e];
      }
      u32x4 o;
#pragma unroll
      for (int j = 0; j < 4; ++j) o[j] = (unsigned)v[2 * j] | ((unsigned)v[2 * j + 1] << 16);
      *(u32x4*)(wfrag + (f * 64 + lane) * 8) = o;
    }
  }
  __syncthreads();
  int bin[4];
  unsigned rank[4];
#pragma unroll
  for (int j = 0; j < 4; ++j) {
    bin[j] = (d[j] >= 0) ? min(d[j] >> 4, P - 1) : -1;
    rank[j] = 0;
    if (bin[j] >= 0) rank[j] = atomicAdd(&hist[bin[j]], 1u);
  }
  __syncthreads();
  u32x4 h = ((u32x4*)hist)[t];
  const unsigned sum = h[0] + h[1] + h[2] + h[3];
  const unsigned incl = wave_incl_scan(sum, lane);
  if (lane == 63) wsum[wv] = incl;
  __syncthreads();
  unsigned base = incl - sum;
#pragma unroll
  for (int k = 0; k < 16; ++k) base += (k < wv) ? wsum[k] : 0u;
  u32x4 o = {base, base + h[0], base + h[0] + h[1], base + h[0] + h[1] + h[2]};
  ((u32x4*)hist)[t] = o;
  if (i0 < pitch) {
    u32x2 pk = {o[0] | (o[1] << 16), o[2] | (o[3] << 16)};
    *(u32x2*)(tab + (size_t)b * pitch + i0) = pk;
  }
  __syncthreads();
#pragma unroll
  for (int j = 0; j < 4; ++j) {
    if (bin[j] >= 0) {
      u32x2 r = {(unsigned)s[j] * 128u, ((unsigned)(d[j] & 15) << 16) | wt[j]};
      lrec[hist[bin[j]] + rank[j]] = r;
    }
  }
  __syncthreads();
  for (int i = 2 * t; i < cnt; i += 2 * K1_THREADS) {
    if (i + 2 <= cnt) {
      *(u32x4*)(recs + e0 + i) = *(const u32x4*)(lrec + i);
    } else {
      recs[e0 + i] = lrec[i];
    }
  }
}

__device__ __forceinline__ void dma4x16(unsigned m0base, unsigned v0, unsigned v1, unsigned v2,
                                        unsigned v3, __amdgpu_buffer_rsrc_t srd) {
  unsigned keep;
  asm volatile(
      "s_waitcnt lgkmcnt(0)\n\t"
      "s_mov_b32 %0, m0\n\t"
      "s_mov_b32 m0, %1\n\t"
      "s_nop 0\n\t"
      "buffer_load_dwordx4 %2, %6, 0 offen lds\n\t"
      "s_add_u32 m0, %1, 1024\n\t"
      "s_nop 0\n\t"
      "buffer_load_dwordx4 %3, %6, 0 offen lds\n\t"
      "s_add_u32 m0, %1, 2048\n\t"
      "s_nop 0\n\t"
      "buffer_load_dwordx4 %4, %6, 0 offen lds\n\t"
      "s_add_u32 m0, %1, 3072\n\t"
      "s_nop 0\n\t"
      "buffer_load_dwordx4 %5, %6, 0 offen lds\n\t"
      "s_mov_b32 m0, %0"
      : "=&s"(keep)
      : "s"(m0base), "v"(v0), "v"(v1), "v"(v2), "v"(v3), "s"(srd)
      : "memory", "scc");
}

__device__ __forceinline__ void dma_rec(unsigned m0a, unsigned m0b, unsigned va, unsigned vb,
                                        __amdgpu_buffer_rsrc_t srd) {
  unsigned keep;
  asm volatile(
      "s_waitcnt lgkmcnt(0)\n\t"
      "s_mov_b32 %0, m0\n\t"
      "s_mov_b32 m0, %1\n\t"
      "s_nop 0\n\t"
      "buffer_load_dword %3, %5, 0 offen lds\n\t"
      "s_mov_b32 m0, %2\n\t"
      "s_nop 0\n\t"
      "buffer_load_dword %4, %5, 0 offen lds\n\t"
      "s_mov_b32 m0, %0"
      : "=&s"(keep)
      : "s"(m0a), "s"(m0b), "v"(va), "v"(vb), "s"(srd)
      : "memory");
}

__device__ __forceinline__ bf16x8 bload16(__amdgpu_buffer_rsrc_t srd, unsigned voff, unsigned soff) {
  u32x4 v = __builtin_amdgcn_raw_buffer_load_b128(srd, voff, soff, 0);
  return __builtin_bit_cast(bf16x8, v);
}

__global__ __launch_bounds__(K2_WAVES * 64) void k_aggr(
    const uint16_t* __restrict__ x, const u32x2* __restrict__ recs,
    const uint16_t* __restrict__ tab, const uint16_t* __restrict__ wfrag,
    uint16_t* __restrict__ out, int N, int T, int B1, int CH, int pitch, int E) {
  __shared__ __attribute__((aligned(16))) char smem[K2_WAVES * WAVE_LDS];
  const int lane = threadIdx.x & 63;
  const int wv = __builtin_amdgcn_readfirstlane(threadIdx.x >> 6);
  const int G = gridDim.x;
  const int bid = blockIdx.x;
  const int lb = (G % 8 == 0) ? (bid % 8) * (G / 8) + bid / 8 : bid;
  const int t_begin = (int)(((long long)lb * T) / G);
  const int t_end = (int)(((long long)(lb + 1) * T) / G);

  char* wbase = smem + wv * WAVE_LDS;
  unsigned* laddr = (unsigned*)wbase;
  unsigned* lsrc = (unsigned*)(wbase + 8192);
  unsigned* lmeta = (unsigned*)(wbase + 8192 + 4 * CAP);
  const unsigned wbase_a = __builtin_amdgcn_readfirstlane((unsigned)(size_t)(LAS char*)wbase);

  const __amdgpu_buffer_rsrc_t srd_x =
      __builtin_amdgcn_make_buffer_rsrc((void*)x, (short)0, N * 128, 0x00020000);
  const __amdgpu_buffer_rsrc_t srd_r =
      __builtin_amdgcn_make_buffer_rsrc((void*)recs, (short)0, E * 8, 0x00020000);
  const __amdgpu_buffer_rsrc_t srd_w =
      __builtin_amdgcn_make_buffer_rsrc((void*)wfrag, (short)0, 16384, 0x00020000);
  const __amdgpu_buffer_rsrc_t srd_t =
      __builtin_amdgcn_make_buffer_rsrc((void*)tab, (short)0, B1 * pitch * 2, 0x00020000);
  const __amdgpu_buffer_rsrc_t srd_o =
      __builtin_amdgcn_make_buffer_rsrc((void*)out, (short)0, N * 128, 0x00020000);

  const int q = lane >> 4, i16 = lane & 15;
  const unsigned ch_even = (unsigned)((lane & 7) ^ (((lane >> 4) & 1) << 1)) << 4;
  const int qp = i16 >> 2, pp = lane & 3;
  const int gsw = ((qp >> 1) & 1) | ((q & 1) << 1);
  const unsigned abase = (unsigned)((8 * q + qp) * 128 + (pp >> 1) * 16 + 8 * (pp & 1));

  for (int tile = t_begin + wv; tile < t_end; tile += K2_WAVES) {
    const unsigned rowoff = (unsigned)(tile * 16 + i16) * 128u + 16u * q;
    f32x4 acc[4];
#pragma unroll
    for (int t = 0; t < 4; ++t) acc[t] = f32x4{0.f, 0.f, 0.f, 0.f};
    f32x4 o[4];
#pragma unroll
    for (int u = 0; u < 4; ++u) o[u] = f32x4{0.f, 0.f, 0.f, 0.f};
    bf16x8 wf[8];

    for (unsigned win = 0;; win += CAP) {
      unsigned run = 0;
      for (int k0 = 0; k0 < B1; k0 += 256) {
        unsigned se[4];
#pragma unroll
        for (int j = 0; j < 4; ++j) {
          const int bb = k0 + j * 64 + lane;
          const unsigned toff = (bb < B1) ? ((unsigned)bb * (unsigned)pitch + (unsigned)tile) * 2u : 0x80000000u;
          const unsigned a = __builtin_amdgcn_raw_buffer_load_b16(srd_t, toff, 0, 0);
          const unsigned e = __builtin_amdgcn_raw_buffer_load_b16(srd_t, toff + 2u, 0, 0);
          se[j] = a | ((e - a) << 16);
        }
        if (win == 0 && k0 == 0) {
#pragma unroll
          for (int f = 0; f < 8; ++f) wf[f] = bload16(srd_w, lane * 16u, f * 1024u);
        }
        const unsigned c = (se[0] >> 16) + (se[1] >> 16) + (se[2] >> 16) + (se[3] >> 16);
        const unsigned incl = wave_incl_scan(c, lane);
        const unsigned tot = __builtin_amdgcn_readlane(incl, 63);
        unsigned pos = run + incl - c - win;
#pragma unroll
        for (int j = 0; j < 4; ++j) {
          const unsigned st = (unsigned)(k0 + j * 64 + lane) * (unsigned)CH + (se[j] & 0xffffu);
          const unsigned cn = se[j] >> 16;
#pragma clang loop unroll(disable) vectorize(disable) interleave(disable)
          for (unsigned i = 0; i < cn; ++i) {
            const unsigned vp = pos + i;
            if (vp < (unsigned)CAP) laddr[vp] = st + i;
          }
          pos += cn;
        }
        run += tot;
      }
      const unsigned M = run;
      const unsigned m = min(M - win, (unsigned)CAP);
      __builtin_amdgcn_wave_barrier();
      const int nrd = (int)((m + 63) >> 6);
#pragma clang loop unroll(disable)
      for (int r = 0; r < nrd; ++r) {
        const unsigned ra = laddr[r * 64 + lane];
        const unsigned va = ((unsigned)(r * 64 + lane) < m) ? ra * 8u : 0x80000000u;
        dma_rec(wbase_a + 8192 + r * 256, wbase_a + 8192 + 4 * CAP + r * 256, va, va + 4u, srd_r);
      }
      if (win == 0) {
        bf16x8 xf[2];
        xf[0] = bload16(srd_x, rowoff, 0);
        xf[1] = bload16(srd_x, rowoff, 64);
        asm volatile("s_waitcnt vmcnt(0)" ::: "memory");
#pragma unroll
        for (int kk = 0; kk < 2; ++kk)
#pragma unroll
          for (int u = 0; u < 4; ++u)
            o[u] = __builtin_amdgcn_mfma_f32_16x16x32_bf16(wf[kk * 4 + u], xf[kk], o[u], 0, 0, 0);
#pragma unroll
        for (int f = 0; f < 8; ++f) wf[f] = bload16(srd_w, lane * 16u, (8 + f) * 1024u);
      } else {
        asm volatile("s_waitcnt vmcnt(0)" ::: "memory");
      }
      const int ng = (int)((m + 31) >> 5);
      {
        const unsigned pidx = m + lane;
        if (pidx < (unsigned)ng * 32u) { lsrc[pidx] = 0x80000000u; lmeta[pidx] = 0xFFFF0000u; }
      }
      __builtin_amdgcn_wave_barrier();

      auto issue = [&](int g) {
        const unsigned* ls = lsrc + g * 32 + (lane >> 3);
        const unsigned v0 = ls[0] + ch_even, v1 = ls[8] + (ch_even ^ 64u), v2 = ls[16] + ch_even,
                       v3 = ls[24] + (ch_even ^ 64u);
        dma4x16(wbase_a + (g & 1) * 4096, v0, v1, v2, v3, srd_x);
      };
      if (ng > 0) issue(0);
      if (ng > 1) issue(1);
#pragma clang loop unroll(disable)
      for (int g = 0; g < ng; ++g) {
        if (g + 1 < ng) asm volatile("s_waitcnt vmcnt(4)" ::: "memory");
        else asm volatile("s_waitcnt vmcnt(0)" ::: "memory");
        const char* bufp = wbase + (g & 1) * 4096 + abase;
        bf16x8 af[4];
#pragma unroll
        for (int t = 0; t < 4; ++t) {
          const char* ap = bufp + ((t ^ gsw) << 5);
          s16x4 lo = __builtin_amdgcn_ds_read_tr16_b64_v4i16((LAS s16x4*)(ap));
          s16x4 hi = __builtin_amdgcn_ds_read_tr16_b64_v4i16((LAS s16x4*)(ap + 512));
          af[t] = __builtin_shufflevector(lo, hi, 0, 1, 2, 3, 4, 5, 6, 7);
        }
        const u32x4 ma = *(const u32x4*)(lmeta + g * 32 + 8 * q);
        const u32x4 mb = *(const u32x4*)(lmeta + g * 32 + 8 * q + 4);
        unsigned sel[8];
#pragma unroll
        for (int j = 0; j < 4; ++j) {
          sel[j] = ((ma[j] >> 16) == (unsigned)i16) ? (ma[j] & 0xffffu) : 0u;
          sel[4 + j] = ((mb[j] >> 16) == (unsigned)i16) ? (mb[j] & 0xffffu) : 0u;
        }
        u32x4 bp = {sel[0] | (sel[1] << 16), sel[2] | (sel[3] << 16), sel[4] | (sel[5] << 16),
                    sel[6] | (sel[7] << 16)};
        const bf16x8 bfr = __builtin_bit_cast(bf16x8, bp);
#pragma unroll
        for (int t = 0; t < 4; ++t)
          acc[t] = __builtin_amdgcn_mfma_f32_16x16x32_bf16(af[t], bfr, acc[t], 0, 0, 0);
        if (g + 2 < ng) issue(g + 2);
      }
      if (win + CAP >= M) break;
    }

#pragma unroll
    for (int s = 0; s < 2; ++s) {
      u32x4 tp = {pk_bf16(acc[2 * s][0], acc[2 * s][1]), pk_bf16(acc[2 * s][2], acc[2 * s][3]),
                  pk_bf16(acc[2 * s + 1][0], acc[2 * s + 1][1]),
                  pk_bf16(acc[2 * s + 1][2], acc[2 * s + 1][3])};
      const bf16x8 tf = __builtin_bit_cast(bf16x8, tp);
#pragma unroll
      for (int u = 0; u < 4; ++u)
        o[u] = __builtin_amdgcn_mfma_f32_16x16x32_bf16(wf[s * 4 + u], tf, o[u], 0, 0, 0);
    }
#pragma unroll
    for (int hh = 0; hh < 2; ++hh) {
      u32x4 ov = {pk_bf16(o[2 * hh][0], o[2 * hh][1]), pk_bf16(o[2 * hh][2], o[2 * hh][3]),
                  pk_bf16(o[2 * hh + 1][0], o[2 * hh + 1][1]),
                  pk_bf16(o[2 * hh + 1][2], o[2 * hh + 1][3])};
      __builtin_amdgcn_raw_buffer_store_b128(ov, srd_o, rowoff + 64u * hh, 0, 0);
    }
  }
}

__device__ __forceinline__ float bf2f(uint16_t v) { return __uint_as_float((unsigned)v << 16); }

__global__ void fb_zero(float* t1, size_t n) {
  for (size_t i = blockIdx.x * (size_t)blockDim.x + threadIdx.x; i < n; i += (size_t)gridDim.x * blockDim.x)
    t1[i] = 0.f;
}
__global__ void fb_scatter(const uint16_t* x, const int* src, const int* dst, const uint16_t* ew,
                           float* t1, int E) {
  const int lane = threadIdx.x & 63;
  const size_t wid = (blockIdx.x * (size_t)blockDim.x + threadIdx.x) >> 6;
  const size_t nw = ((size_t)gridDim.x * blockDim.x) >> 6;
  for (size_t e = wid; e < (size_t)E; e += nw)
    atomicAdd(&t1[(size_t)dst[e] * DF + lane], bf2f(ew[e]) * bf2f(x[(size_t)src[e] * DF + lane]));
}
__global__ void fb_out(const uint16_t* x, const float* t1, const uint16_t* w, uint16_t* out, int N) {
  const int lane = threadIdx.x & 63;
  const size_t wid = (blockIdx.x * (size_t)blockDim.x + threadIdx.x) >> 6;
  const size_t nw = ((size_t)gridDim.x * blockDim.x) >> 6;
  for (size_t n = wid; n < (size_t)N; n += nw) {
    float a = 0.f;
    for (int d = 0; d < DF; ++d) {
      const float tv = bf2f((uint16_t)(pk_bf16(t1[n * DF + d], 0.f) & 0xffffu));
      a += bf2f(x[n * DF + d]) * bf2f(w[d * DF + lane]) + tv * bf2f(w[(DF + d) * DF + lane]);
    }
    out[n * DF + lane] = (uint16_t)(pk_bf16(a, 0.f) & 0xffffu);
  }
}

extern "C" void kernel_launch(void* const* d_in, const int* in_sizes, int n_in, void* d_out,
                              int out_size, void* d_ws, size_t ws_size, hipStream_t stream) {
  const uint16_t* x = (const uint16_t*)d_in[0];
  const int* src = (const int*)d_in[1];
  const int* dst = (const int*)d_in[2];
  const uint16_t* ew = (const uint16_t*)d_in[3];
  const uint16_t* w = (const uint16_t*)d_in[4];
  uint16_t* out = (uint16_t*)d_out;
  const int N = in_sizes[0] / DF;
  const int E = in_sizes[1];
  const int P = (N + 15) / 16;

  int CH = (E + 255) / 256;
  CH = (CH + 3) & ~3;
  if (CH < 4) CH = 4;
  const bool fast = (P + 1 <= MAX_BINS) && (CH <= MAX_CH) && (E > 0) && ((size_t)E * 8 < 0x7fffffffull);
  if (fast) {
    const int B1 = (E + CH - 1) / CH;
    const int pitch = (P + 1 + 63) & ~63;
    char* ws = (char*)d_ws;
    size_t off = 0;
    u32x2* recs = (u32x2*)(ws + off);
    off += ((size_t)E * 8 + 255) & ~(size_t)255;
    uint16_t* tab = (uint16_t*)(ws + off);
    off += ((size_t)B1 * pitch * 2 + 255) & ~(size_t)255;
    uint16_t* wfrag = (uint16_t*)(ws + off);
    k_part<<<B1, K1_THREADS, 0, stream>>>(src, dst, ew, w, recs, tab, wfrag, E, CH, P, pitch);
    k_aggr<<<K2_GRID, K2_WAVES * 64, 0, stream>>>(x, recs, tab, wfrag, out, N, P, B1, CH, pitch, E);
  } else {
    float* t1 = (float*)d_ws;
    fb_zero<<<2048, 256, 0, stream>>>(t1, (size_t)N * DF);
    if (E > 0) fb_scatter<<<2048, 256, 0, stream>>>(x, src, dst, ew, t1, E);
    fb_out<<<2048, 256, 0, stream>>>(x, t1, w, out, N);
  }
}
```

```cpp
#include <hip/hip_runtime.h>
#include <hip/hip_bf16.h>
#include <stdint.h>

typedef __attribute__((ext_vector_type(4))) short s16x4;
typedef __attribute__((ext_vector_type(8))) short bf16x8;
typedef __attribute__((ext_vector_type(4))) float f32x4;
typedef __attribute__((ext_vector_type(2))) float f32x2;
typedef __attribute__((ext_vector_type(2))) __bf16 bf16x2v;
typedef __attribute__((ext_vector_type(4))) unsigned u32x4;
typedef __attribute__((ext_vector_type(2))) unsigned u32x2;
typedef __attribute__((ext_vector_type(4))) int i32x4;
typedef __attribute__((ext_vector_type(4))) unsigned short u16x4;
#define LAS __attribute__((address_space(3)))

#define STAMP_DECL
#define STAMP(i)
#define STAMP_OUT(ptr, idx)

constexpr int DF = 64;
constexpr int K1_THREADS = 1024;
constexpr int MAX_BINS = 4096;
constexpr int MAX_CH = 4096;
constexpr int K2_WAVES = 13;
constexpr int K2_GRID = 256;
constexpr int CAP = 512;

__device__ __forceinline__ unsigned pk_bf16(float a, float b) {
  f32x2 v = {a, b};
  bf16x2v r = __builtin_convertvector(v, bf16x2v);
  return __builtin_bit_cast(unsigned, r);
}

__device__ __forceinline__ unsigned wave_incl_scan(unsigned v, int  ) {
  v += __builtin_amdgcn_update_dpp(0u, v, 0x111, 0xf, 0xf, false);
  v += __builtin_amdgcn_update_dpp(0u, v, 0x112, 0xf, 0xf, false);
  v += __builtin_amdgcn_update_dpp(0u, v, 0x114, 0xf, 0xf, false);
  v += __builtin_amdgcn_update_dpp(0u, v, 0x118, 0xf, 0xf, false);
  v += __builtin_amdgcn_update_dpp(0u, v, 0x142, 0xa, 0xf, false);
  v += __builtin_amdgcn_update_dpp(0u, v, 0x143, 0xc, 0xf, false);
  return v;
}

__device__ __forceinline__ int tile_begin(int lb, int q, int r) {
  return lb < r ? lb * (q + 1) : r * (q + 1) + (lb - r) * q;
}

__global__ __launch_bounds__(K1_THREADS) void k_part(
    const int* __restrict__ src, const int* __restrict__ dst, const uint16_t* __restrict__ ew,
    const uint16_t* __restrict__ w, u32x2* __restrict__ recs, uint16_t* __restrict__ tab,
    uint16_t* __restrict__ wfrag, int E, int CH, int P, int G2, unsigned mg0, unsigned mg1) {
  __shared__ __attribute__((aligned(16))) unsigned hist[MAX_BINS];
  __shared__ __attribute__((aligned(16))) u32x2 lrec[MAX_CH];
  __shared__ unsigned wsum[16];
  const int b = blockIdx.x, t = threadIdx.x, lane = t & 63, wv = t >> 6;
  STAMP_DECL
  const int tq = P / G2, tr = P % G2;
  const unsigned split = (unsigned)(tr * (tq + 1));
  const int e0 = b * CH;
  const int cnt = min(CH, E - e0);
  const int i0 = 4 * t;
  int d[4], s[4];
  unsigned wt[4];
  if (i0 + 4 <= cnt) {
    i32x4 d4 = *(const i32x4*)(dst + e0 + i0);
    i32x4 s4 = *(const i32x4*)(src + e0 + i0);
    u16x4 w4 = *(const u16x4*)(ew + e0 + i0);
#pragma unroll
    for (int j = 0; j < 4; ++j) { d[j] = d4[j]; s[j] = s4[j]; wt[j] = w4[j]; }
  } else {
#pragma unroll
    for (int j = 0; j < 4; ++j) {
      bool ok = i0 + j < cnt;
      d[j] = ok ? dst[e0 + i0 + j] : -1;
      s[j] = ok ? src[e0 + i0 + j] : 0;
      wt[j] = ok ? ew[e0 + i0 + j] : 0;
    }
  }
  ((u32x4*)hist)[t] = u32x4{0u, 0u, 0u, 0u};
  if (wv == 15) {
    for (int f = b; f < 16; f += gridDim.x) {
      const int kk = f >> 2, u = f & 3, i = lane & 15, q = lane >> 4;
      const int e = 32 * (u >> 1) + 8 * (i >> 2) + 4 * (u & 1) + (i & 3);
      unsigned short v[8];
#pragma unroll
      for (int j = 0; j < 8; ++j) {
        int dd, kw;
        if (kk < 2) { kw = 0; dd = 32 * kk + 8 * q + j; }
        else { kw = 1; dd = 16 * (2 * (kk - 2) + (j >> 2)) + 4 * q + (j & 3); }
        v[j] = w[(kw * DF + dd) * DF + e];
      }
      u32x4 o;
#pragma unroll
      for (int j = 0; j < 4; ++j) o[j] = (unsigned)v[2 * j] | ((unsigned)v[2 * j + 1] << 16);
      *(u32x4*)(wfrag + (f * 64 + lane) * 8) = o;
    }
  }
  __syncthreads();
  STAMP(1)
  int bin[4];
  unsigned rank[4];
#pragma unroll
  for (int j = 0; j < 4; ++j) {
    bin[j] = (d[j] >= 0) ? min(d[j] >> 4, P - 1) : -1;
    rank[j] = 0;
    if (bin[j] >= 0) rank[j] = atomicAdd(&hist[bin[j]], 1u);
  }
  __syncthreads();
  STAMP(2)
  u32x4 h = ((u32x4*)hist)[t];
  const unsigned sum = h[0] + h[1] + h[2] + h[3];
  const unsigned incl = wave_incl_scan(sum, lane);
  if (lane == 63) wsum[wv] = incl;
  __syncthreads();
  unsigned base = incl - sum;
#pragma unroll
  for (int k = 0; k < 16; ++k) base += (k < wv) ? wsum[k] : 0u;
  u32x4 o = {base, base + h[0], base + h[0] + h[1], base + h[0] + h[1] + h[2]};
  ((u32x4*)hist)[t] = o;
  __syncthreads();
  STAMP(3)
#pragma unroll
  for (int j = 0; j < 4; ++j) {
    if (bin[j] >= 0) {
      const unsigned tl = (unsigned)bin[j];
      const bool lowp = tl < split;
      const unsigned xx = lowp ? tl : tl - split;
      const unsigned dq = (xx * (lowp ? mg1 : mg0)) >> 20;
      const unsigned widx = xx - dq * (unsigned)(lowp ? tq + 1 : tq);
      u32x2 r = {(unsigned)s[j] * 128u, (widx << 20) | ((unsigned)(d[j] & 15) << 16) | wt[j]};
      lrec[hist[bin[j]] + rank[j]] = r;
    }
  }
  __syncthreads();
  STAMP(4)
  for (int u = t; u < 2 * G2; u += K1_THREADS) {
    const int lb = u >> 1, hf = u & 1;
    const int t0 = tile_begin(lb, tq, tr) + 8 * hf;
    unsigned v[8];
#pragma unroll
    for (int j = 0; j < 8; ++j) v[j] = hist[min(t0 + j, P)];
    u32x4 pk = {v[0] | (v[1] << 16), v[2] | (v[3] << 16), v[4] | (v[5] << 16), v[6] | (v[7] << 16)};
    *(u32x4*)(tab + (((size_t)lb * gridDim.x + b) * 16 + 8 * hf)) = pk;
  }
  STAMP(5)
  for (int i = 2 * t; i < cnt; i += 2 * K1_THREADS) {
    if (i + 2 <= cnt) {
      *(u32x4*)(recs + e0 + i) = *(const u32x4*)(lrec + i);
    } else {
      recs[e0 + i] = lrec[i];
    }
  }
  STAMP_OUT((char*)wfrag + 65536 + 1048576, b * 16 + wv)
}

__device__ __forceinline__ void dma4x16(unsigned m0base, unsigned v0, unsigned v1, unsigned v2,
                                        unsigned v3, __amdgpu_buffer_rsrc_t srd) {
  unsigned keep;
  asm volatile(
      "s_waitcnt lgkmcnt(0)\n\t"
      "s_mov_b32 %0, m0\n\t"
      "s_mov_b32 m0, %1\n\t"
      "s_nop 0\n\t"
      "buffer_load_dwordx4 %2, %6, 0 offen lds\n\t"
      "s_add_u32 m0, %1, 1024\n\t"
      "s_nop 0\n\t"
      "buffer_load_dwordx4 %3, %6, 0 offen lds\n\t"
      "s_add_u32 m0, %1, 2048\n\t"
      "s_nop 0\n\t"
      "buffer_load_dwordx4 %4, %6, 0 offen lds\n\t"
      "s_add_u32 m0, %1, 3072\n\t"
      "s_nop 0\n\t"
      "buffer_load_dwordx4 %5, %6, 0 offen lds\n\t"
      "s_mov_b32 m0, %0"
      : "=&s"(keep)
      : "s"(m0base), "v"(v0), "v"(v1), "v"(v2), "v"(v3), "s"(srd)
      : "memory", "scc");
}

__device__ __forceinline__ void dma_rec(unsigned m0a, unsigned m0b, unsigned va, unsigned vb,
                                        __amdgpu_buffer_rsrc_t srd) {
  unsigned keep;
  asm volatile(
      "s_waitcnt lgkmcnt(0)\n\t"
      "s_mov_b32 %0, m0\n\t"
      "s_mov_b32 m0, %1\n\t"
      "s_nop 0\n\t"
      "buffer_load_dword %3, %5, 0 offen lds\n\t"
      "s_mov_b32 m0, %2\n\t"
      "s_nop 0\n\t"
      "buffer_load_dword %4, %5, 0 offen lds\n\t"
      "s_mov_b32 m0, %0"
      : "=&s"(keep)
      : "s"(m0a), "s"(m0b), "v"(va), "v"(vb), "s"(srd)
      : "memory");
}

__device__ __forceinline__ bf16x8 bload16(__amdgpu_buffer_rsrc_t srd, unsigned voff, unsigned soff) {
  u32x4 v = __builtin_amdgcn_raw_buffer_load_b128(srd, voff, soff, 0);
  return __builtin_bit_cast(bf16x8, v);
}

__device__ __forceinline__ void dma1x16(unsigned m0base, unsigned v0, __amdgpu_buffer_rsrc_t srd) {
  unsigned keep;
  asm volatile(
      "s_waitcnt lgkmcnt(0)\n\t"
      "s_mov_b32 %0, m0\n\t"
      "s_mov_b32 m0, %1\n\t"
      "s_nop 0\n\t"
      "buffer_load_dwordx4 %2, %3, 0 offen lds\n\t"
      "s_mov_b32 m0, %0"
      : "=&s"(keep)
      : "s"(m0base), "v"(v0), "s"(srd)
      : "memory");
}

constexpr int LDS_LISTS = K2_WAVES * 8192;
constexpr int LDS_TOTAL = LDS_LISTS + K2_WAVES * 8 * CAP;

__global__ __launch_bounds__(K2_WAVES * 64) void k_aggr(
    const uint16_t* __restrict__ x, const u32x2* __restrict__ recs,
    const uint16_t* __restrict__ tab, const uint16_t* __restrict__ wfrag,
    uint16_t* __restrict__ out, int N, int T, int B1, int CH, int E) {
  __shared__ __attribute__((aligned(16))) char smem[LDS_TOTAL];
  const int lane = threadIdx.x & 63;
  const int wv = __builtin_amdgcn_readfirstlane(threadIdx.x >> 6);
  const int G = gridDim.x;
  STAMP_DECL
  const int bid = blockIdx.x;
  const int lb = (G % 8 == 0) ? (bid % 8) * (G / 8) + bid / 8 : bid;
  const int tq = T / G, tr = T % G;
  const int t_begin = tile_begin(lb, tq, tr);
  const int nt = tq + (lb < tr ? 1 : 0);
  const int tile = t_begin + wv;
  const bool active = wv < nt;

  char* rbuf = smem + wv * 8192;
  unsigned* lists = (unsigned*)(smem + LDS_LISTS);
  unsigned* lsrc = lists + wv * 2 * CAP;
  unsigned* lmeta = lsrc + CAP;
  const uint16_t* tabL = (const uint16_t*)smem;
  int* adjL = (int*)(smem + 8192);
  unsigned* cntL = (unsigned*)(smem + 24576);
  const unsigned smem_a = (unsigned)(size_t)(LAS char*)smem;
  const unsigned rbuf_a = smem_a + wv * 8192;

  const __amdgpu_buffer_rsrc_t srd_x =
      __builtin_amdgcn_make_buffer_rsrc((void*)x, (short)0, N * 128, 0x00020000);
  const __amdgpu_buffer_rsrc_t srd_r =
      __builtin_amdgcn_make_buffer_rsrc((void*)recs, (short)0, E * 8, 0x00020000);
  const __amdgpu_buffer_rsrc_t srd_w =
      __builtin_amdgcn_make_buffer_rsrc((void*)wfrag, (short)0, 16384, 0x00020000);
  const __amdgpu_buffer_rsrc_t srd_t = __builtin_amdgcn_make_buffer_rsrc(
      (void*)(tab + (size_t)lb * B1 * 16), (short)0, B1 * 32, 0x00020000);
  const __amdgpu_buffer_rsrc_t srd_o =
      __builtin_amdgcn_make_buffer_rsrc((void*)out, (short)0, N * 128, 0x00020000);

  const int q = lane >> 4, i16 = lane & 15;
  const unsigned ch_even = (unsigned)((lane & 7) ^ (((lane >> 4) & 1) << 1)) << 4;
  const int qp = i16 >> 2, pp = lane & 3;
  const int gsw = ((qp >> 1) & 1) | ((q & 1) << 1);
  const unsigned abase = (unsigned)((8 * q + qp) * 128 + (pp >> 1) * 16 + 8 * (pp & 1));

  const unsigned rowoff = active ? (unsigned)(tile * 16 + i16) * 128u + 16u * q : 0x80000000u;
  f32x4 acc[4];
#pragma unroll
  for (int t = 0; t < 4; ++t) acc[t] = f32x4{0.f, 0.f, 0.f, 0.f};
  f32x4 o[4];
#pragma unroll
  for (int u = 0; u < 4; ++u) o[u] = f32x4{0.f, 0.f, 0.f, 0.f};
  bf16x8 wf[8];
  bool first = true;

  for (int cg = 0; cg < B1; cg += 256) {
    const int nb = min(256, B1 - cg);
    for (unsigned rnd = 0;; rnd += CAP) {
      if (!first) __syncthreads();
      if (wv * 32 < nb) dma1x16(smem_a + wv * 1024, (unsigned)(cg * 32 + wv * 1024 + lane * 16), srd_t);
      if (first) {
        bf16x8 xf[2];
#pragma unroll
        for (int f = 0; f < 8; ++f) wf[f] = bload16(srd_w, lane * 16u, f * 1024u);
        xf[0] = bload16(srd_x, rowoff, 0);
        xf[1] = bload16(srd_x, rowoff, 64);
        asm volatile("s_waitcnt vmcnt(10)" ::: "memory");
        __syncthreads();
#pragma unroll
        for (int kk = 0; kk < 2; ++kk)
#pragma unroll
          for (int u = 0; u < 4; ++u)
            o[u] = __builtin_amdgcn_mfma_f32_16x16x32_bf16(wf[kk * 4 + u], xf[kk], o[u], 0, 0, 0);
        first = false;
      } else {
        asm volatile("s_waitcnt vmcnt(0)" ::: "memory");
        __syncthreads();
      }
      STAMP(1)
      {
        int ln = lane;
        asm volatile("" : "+v"(ln));
        unsigned cn[4], of[4];
#pragma unroll
        for (int j = 0; j < 4; ++j) {
          const int bb = j * 64 + ln;
          const u32x4* trw = (const u32x4*)(tabL + min(bb, nb - 1) * 16);
          const u32x4 r0 = trw[0], r1 = trw[1];
          unsigned lo = r0[0], hi = r0[1];
          switch (wv >> 1) {
            case 1: lo = r0[1]; hi = r0[2]; break;
            case 2: lo = r0[2]; hi = r0[3]; break;
            case 3: lo = r0[3]; hi = r1[0]; break;
            case 4: lo = r1[0]; hi = r1[1]; break;
            case 5: lo = r1[1]; hi = r1[2]; break;
            case 6: lo = r1[2]; hi = r1[3]; break;
            default: break;
          }
          const unsigned pr = (wv & 1) ? __builtin_amdgcn_alignbit(hi, lo, 16) : lo;
          const unsigned a0 = r0[0] & 0xffffu, aw = pr & 0xffffu, aw1 = pr >> 16;
          cn[j] = (bb < nb && active) ? aw1 - aw : 0u;
          of[j] = aw - a0;
        }
        const unsigned c = cn[0] + cn[1] + cn[2] + cn[3];
        const unsigned incl = wave_incl_scan(c, ln);
        unsigned p = incl - c - rnd;
#pragma unroll
        for (int j = 0; j < 4; ++j) {
          adjL[wv * 256 + j * 64 + ln] = (int)(p - of[j]);
          p += cn[j];
        }
        if (ln == 63) cntL[wv] = incl;
      }
      __syncthreads();
      STAMP(2)
      unsigned mx = 0;
#pragma unroll
      for (int k = 0; k < K2_WAVES; ++k) mx = max(mx, cntL[k]);
      mx = __builtin_amdgcn_readfirstlane(mx);
      const unsigned mine = __builtin_amdgcn_readfirstlane(cntL[wv]);
      {
        int tid = threadIdx.x;
        asm volatile("" : "+v"(tid));
        u32x4 rv[6];
        unsigned lens[3], goff[3];
        int scs[3];
#pragma unroll
        for (int sw = 0; sw < 3; ++sw) {
          const int sg = (tid >> 3) + sw * (K2_WAVES * 8);
          const int sc = min(sg, nb - 1);
          scs[sw] = sc;
          const unsigned a0 = tabL[sc * 16];
          lens[sw] = (sg < nb) ? (unsigned)tabL[sc * 16 + nt] - a0 : 0u;
          goff[sw] = ((unsigned)(cg + sc) * (unsigned)CH + a0) * 8u;
#pragma unroll
          for (int ps = 0; ps < 2; ++ps) {
            const unsigned oo = (unsigned)(tid & 7) * 2u + 16u * ps;
            rv[sw * 2 + ps] = __builtin_amdgcn_raw_buffer_load_b128(
                srd_r, (oo < lens[sw]) ? goff[sw] + oo * 8u : 0x80000000u, 0, 0);
          }
        }
        auto place = [&](int sc, unsigned oo, unsigned rs, unsigned rm) {
          const unsigned w = (rm >> 20) & 15u;
          const unsigned slot = (unsigned)(adjL[w * 256 + sc] + (int)oo);
          if (slot < (unsigned)CAP) {
            lists[w * 2 * CAP + slot] = rs;
            lists[w * 2 * CAP + CAP + slot] = rm & 0xFFFFFu;
          }
        };
#pragma unroll
        for (int sw = 0; sw < 3; ++sw) {
#pragma unroll
          for (int ps = 0; ps < 2; ++ps) {
            const unsigned oo = (unsigned)(tid & 7) * 2u + 16u * ps;
            const u32x4 r = rv[sw * 2 + ps];
            if (oo < lens[sw]) place(scs[sw], oo, r[0], r[1]);
            if (oo + 1 < lens[sw]) place(scs[sw], oo + 1, r[2], r[3]);
          }
        }
#pragma unroll
        for (int sw = 0; sw < 3; ++sw) {
#pragma clang loop unroll(disable) vectorize(disable) interleave(disable)
          for (unsigned oo = (unsigned)(tid & 7) + 32u; oo < lens[sw]; oo += 8u) {
            const u32x2 r = __builtin_amdgcn_raw_buffer_load_b64(srd_r, goff[sw] + oo * 8u, 0, 0);
            place(scs[sw], oo, r[0], r[1]);
          }
        }
      }
      STAMP(3)
      const unsigned m = (mine > rnd) ? min(mine - rnd, (unsigned)CAP) : 0u;
      const int ng = (int)((m + 31) >> 5);
      {
        const unsigned pidx = m + lane;
        if (pidx < (unsigned)ng * 32u) { lsrc[pidx] = 0x80000000u; lmeta[pidx] = 0xFFFF0000u; }
      }
#pragma unroll
      for (int f = 0; f < 8; ++f) wf[f] = bload16(srd_w, lane * 16u, (8 + f) * 1024u);
      __syncthreads();
      STAMP(4)
      auto issue = [&](int g) {
        const unsigned* ls = lsrc + g * 32 + (lane >> 3);
        const unsigned v0 = ls[0] + ch_even, v1 = ls[8] + (ch_even ^ 64u), v2 = ls[16] + ch_even,
                       v3 = ls[24] + (ch_even ^ 64u);
        dma4x16(rbuf_a + (g & 1) * 4096, v0, v1, v2, v3, srd_x);
      };
      if (ng > 0) issue(0);
      if (ng > 1) issue(1);
#pragma clang loop unroll(disable)
      for (int g = 0; g < ng; ++g) {
        if (g + 1 < ng) asm volatile("s_waitcnt vmcnt(4)" ::: "memory");
        else asm volatile("s_waitcnt vmcnt(0)" ::: "memory");
        const char* bufp = rbuf + (g & 1) * 4096 + abase;
        bf16x8 af[4];
#pragma unroll
        for (int t = 0; t < 4; ++t) {
          const char* ap = bufp + ((t ^ gsw) << 5);
          s16x4 lo = __builtin_amdgcn_ds_read_tr16_b64_v4i16((LAS s16x4*)(ap));
          s16x4 hi = __builtin_amdgcn_ds_read_tr16_b64_v4i16((LAS s16x4*)(ap + 512));
          af[t] = __builtin_shufflevector(lo, hi, 0, 1, 2, 3, 4, 5, 6, 7);
        }
        const u32x4 ma = *(const u32x4*)(lmeta + g * 32 + 8 * q);
        const u32x4 mb = *(const u32x4*)(lmeta + g * 32 + 8 * q + 4);
        unsigned sel[8];
#pragma unroll
        for (int j = 0; j < 4; ++j) {
          sel[j] = ((ma[j] >> 16) == (unsigned)i16) ? (ma[j] & 0xffffu) : 0u;
          sel[4 + j] = ((mb[j] >> 16) == (unsigned)i16) ? (mb[j] & 0xffffu) : 0u;
        }
        u32x4 bp = {sel[0] | (sel[1] << 16), sel[2] | (sel[3] << 16), sel[4] | (sel[5] << 16),
                    sel[6] | (sel[7] << 16)};
        const bf16x8 bfr = __builtin_bit_cast(bf16x8, bp);
#pragma unroll
        for (int t = 0; t < 4; ++t)
          acc[t] = __builtin_amdgcn_mfma_f32_16x16x32_bf16(af[t], bfr, acc[t], 0, 0, 0);
        if (g + 2 < ng) issue(g + 2);
      }
      if (rnd + CAP >= mx) break;
    }
  }

  STAMP(5)
#pragma unroll
  for (int s = 0; s < 2; ++s) {
    u32x4 tp = {pk_bf16(acc[2 * s][0], acc[2 * s][1]), pk_bf16(acc[2 * s][2], acc[2 * s][3]),
                pk_bf16(acc[2 * s + 1][0], acc[2 * s + 1][1]),
                pk_bf16(acc[2 * s + 1][2], acc[2 * s + 1][3])};
    const bf16x8 tf = __builtin_bit_cast(bf16x8, tp);
#pragma unroll
    for (int u = 0; u < 4; ++u)
      o[u] = __builtin_amdgcn_mfma_f32_16x16x32_bf16(wf[s * 4 + u], tf, o[u], 0, 0, 0);
  }
#pragma unroll
  for (int hh = 0; hh < 2; ++hh) {
    u32x4 ov = {pk_bf16(o[2 * hh][0], o[2 * hh][1]), pk_bf16(o[2 * hh][2], o[2 * hh][3]),
                pk_bf16(o[2 * hh + 1][0], o[2 * hh + 1][1]),
                pk_bf16(o[2 * hh + 1][2], o[2 * hh + 1][3])};
    __builtin_amdgcn_raw_buffer_store_b128(ov, srd_o, rowoff + 64u * hh, 0, 0);
  }
  STAMP_OUT((char*)wfrag + 65536, bid * 16 + wv)
}

__device__ __forceinline__ float bf2f(uint16_t v) { return __uint_as_float((unsigned)v << 16); }

__global__ void fb_zero(float* t1, size_t n) {
  for (size_t i = blockIdx.x * (size_t)blockDim.x + threadIdx.x; i < n; i += (size_t)gridDim.x * blockDim.x)
    t1[i] = 0.f;
}
__global__ void fb_scatter(const uint16_t* x, const int* src, const int* dst, const uint16_t* ew,
                           float* t1, int E) {
  const int lane = threadIdx.x & 63;
  const size_t wid = (blockIdx.x * (size_t)blockDim.x + threadIdx.x) >> 6;
  const size_t nw = ((size_t)gridDim.x * blockDim.x) >> 6;
  for (size_t e = wid; e < (size_t)E; e += nw)
    atomicAdd(&t1[(size_t)dst[e] * DF + lane], bf2f(ew[e]) * bf2f(x[(size_t)src[e] * DF + lane]));
}
__global__ void fb_out(const uint16_t* x, const float* t1, const uint16_t* w, uint16_t* out, int N) {
  const int lane = threadIdx.x & 63;
  const size_t wid = (blockIdx.x * (size_t)blockDim.x + threadIdx.x) >> 6;
  const size_t nw = ((size_t)gridDim.x * blockDim.x) >> 6;
  for (size_t n = wid; n < (size_t)N; n += nw) {
    float a = 0.f;
    for (int d = 0; d < DF; ++d) {
      const float tv = bf2f((uint16_t)(pk_bf16(t1[n * DF + d], 0.f) & 0xffffu));
      a += bf2f(x[n * DF + d]) * bf2f(w[d * DF + lane]) + tv * bf2f(w[(DF + d) * DF + lane]);
    }
    out[n * DF + lane] = (uint16_t)(pk_bf16(a, 0.f) & 0xffffu);
  }
}

extern "C" void kernel_launch(void* const* d_in, const int* in_sizes, int n_in, void* d_out,
                              int out_size, void* d_ws, size_t ws_size, hipStream_t stream) {
  const uint16_t* x = (const uint16_t*)d_in[0];
  const int* src = (const int*)d_in[1];
  const int* dst = (const int*)d_in[2];
  const uint16_t* ew = (const uint16_t*)d_in[3];
  const uint16_t* w = (const uint16_t*)d_in[4];
  uint16_t* out = (uint16_t*)d_out;
  const int N = in_sizes[0] / DF;
  const int E = in_sizes[1];
  const int P = (N + 15) / 16;

  int CH = (E + 255) / 256;
  CH = (CH + 3) & ~3;
  if (CH < 4) CH = 4;
  const bool fast = (P + 1 <= MAX_BINS) && (CH <= MAX_CH) && (E > 0) && ((size_t)E * 8 < 0x7fffffffull);
  if (fast) {
    const int B1 = (E + CH - 1) / CH;
    int G2 = K2_GRID;
    while ((long long)G2 * K2_WAVES < (long long)P) G2 += 8;
    char* ws = (char*)d_ws;
    size_t off = 0;
    u32x2* recs = (u32x2*)(ws + off);
    off += ((size_t)E * 8 + 255) & ~(size_t)255;
    uint16_t* tab = (uint16_t*)(ws + off);
    off += ((size_t)B1 * G2 * 32 + 255) & ~(size_t)255;
    uint16_t* wfrag = (uint16_t*)(ws + off);
    const unsigned tq = (unsigned)(P / G2);
    const unsigned mg0 = tq ? ((1u << 20) + tq - 1) / tq : 0u;
    const unsigned mg1 = ((1u << 20) + tq) / (tq + 1);
    k_part<<<B1, K1_THREADS, 0, stream>>>(src, dst, ew, w, recs, tab, wfrag, E, CH, P, G2, mg0, mg1);
    k_aggr<<<G2, K2_WAVES * 64, 0, stream>>>(x, recs, tab, wfrag, out, N, P, B1, CH, E);
  } else {
    float* t1 = (float*)d_ws;
    fb_zero<<<2048, 256, 0, stream>>>(t1, (size_t)N * DF);
    if (E > 0) fb_scatter<<<2048, 256, 0, stream>>>(x, src, dst, ew, t1, E);
    fb_out<<<2048, 256, 0, stream>>>(x, t1, w, out, N);
  }
}
```

```cpp
#include <hip/hip_runtime.h>
#include <hip/hip_bf16.h>
#include <stdint.h>

typedef __attribute__((ext_vector_type(4))) short s16x4;
typedef __attribute__((ext_vector_type(8))) short bf16x8;
typedef __attribute__((ext_vector_type(4))) float f32x4;
typedef __attribute__((ext_vector_type(2))) float f32x2;
typedef __attribute__((ext_vector_type(2))) __bf16 bf16x2v;
typedef __attribute__((ext_vector_type(4))) unsigned u32x4;
typedef __attribute__((ext_vector_type(2))) unsigned u32x2;
typedef __attribute__((ext_vector_type(4))) int i32x4;
typedef __attribute__((ext_vector_type(4))) unsigned short u16x4;
#define LAS __attribute__((address_space(3)))

#define STAMP_DECL
#define STAMP(i)
#define STAMP_OUT(ptr, idx)

constexpr int DF = 64;
constexpr int K1_THREADS = 1024;
constexpr int MAX_BINS = 4096;
constexpr int MAX_CH = 4096;
constexpr int K2_WAVES = 13;
constexpr int K2_GRID = 256;
constexpr int CAP = 512;

__device__ __forceinline__ unsigned pk_bf16(float a, float b) {
  f32x2 v = {a, b};
  bf16x2v r = __builtin_convertvector(v, bf16x2v);
  return __builtin_bit_cast(unsigned, r);
}

__device__ __forceinline__ unsigned wave_incl_scan(unsigned v, int  ) {
  v += __builtin_amdgcn_update_dpp(0u, v, 0x111, 0xf, 0xf, false);
  v += __builtin_amdgcn_update_dpp(0u, v, 0x112, 0xf, 0xf, false);
  v += __builtin_amdgcn_update_dpp(0u, v, 0x114, 0xf, 0xf, false);
  v += __builtin_amdgcn_update_dpp(0u, v, 0x118, 0xf, 0xf, false);
  v += __builtin_amdgcn_update_dpp(0u, v, 0x142, 0xa, 0xf, false);
  v += __builtin_amdgcn_update_dpp(0u, v, 0x143, 0xc, 0xf, false);
  return v;
}

__global__ __launch_bounds__(K1_THREADS) void k_part(
    const int* __restrict__ src, const int* __restrict__ dst, const uint16_t* __restrict__ ew,
    const uint16_t* __restrict__ w, u32x2* __restrict__ recs, uint16_t* __restrict__ tab,
    uint16_t* __restrict__ wfrag, int E, int CH, int P, int G2, int tq, int tr, int B1,
    unsigned mg0, unsigned mg1) {
  __shared__ __attribute__((aligned(16))) unsigned hist[MAX_BINS];
  __shared__ __attribute__((aligned(16))) u32x2 lrec[MAX_CH];
  __shared__ uint16_t lkey[MAX_CH];
  __shared__ unsigned wsum[16];
  asm volatile("" ::"s"(src), "s"(dst), "s"(ew), "s"(w), "s"(recs), "s"(tab), "s"(wfrag), "s"(E),
               "s"(CH), "s"(P), "s"(G2), "s"(tq), "s"(tr), "s"(B1), "s"(mg0), "s"(mg1));
  const int b = blockIdx.x, t = threadIdx.x, lane = t & 63, wv = t >> 6;
  STAMP_DECL
  const unsigned split = (unsigned)(tr * (tq + 1));
  const int e0 = b * CH;
  const int cnt = min(CH, E - e0);
  const int i0 = 4 * t;
  int d[4], s[4];
  unsigned wt[4];
  if (i0 + 4 <= cnt) {
    i32x4 d4 = *(const i32x4*)(dst + e0 + i0);
    i32x4 s4 = *(const i32x4*)(src + e0 + i0);
    u16x4 w4 = *(const u16x4*)(ew + e0 + i0);
#pragma unroll
    for (int j = 0; j < 4; ++j) { d[j] = d4[j]; s[j] = s4[j]; wt[j] = w4[j]; }
  } else {
#pragma unroll
    for (int j = 0; j < 4; ++j) {
      bool ok = i0 + j < cnt;
      d[j] = ok ? dst[e0 + i0 + j] : -1;
      s[j] = ok ? src[e0 + i0 + j] : 0;
      wt[j] = ok ? ew[e0 + i0 + j] : 0;
    }
  }
  ((u32x4*)hist)[t] = u32x4{0u, 0u, 0u, 0u};
  if (wv == 15) {
    for (int f = b; f < 16; f += B1) {
      const int kk = f >> 2, u = f & 3, i = lane & 15, q = lane >> 4;
      const int e = 32 * (u >> 1) + 8 * (i >> 2) + 4 * (u & 1) + (i & 3);
      unsigned short v[8];
#pragma unroll
      for (int j = 0; j < 8; ++j) {
        int dd, kw;
        if (kk < 2) { kw = 0; dd = 32 * kk + 8 * q + j; }
        else { kw = 1; dd = 16 * (2 * (kk - 2) + (j >> 2)) + 4 * q + (j & 3); }
        v[j] = w[(kw * DF + dd) * DF + e];
      }
      u32x4 o;
#pragma unroll
      for (int j = 0; j < 4; ++j) o[j] = (unsigned)v[2 * j] | ((unsigned)v[2 * j + 1] << 16);
      *(u32x4*)(wfrag + (f * 64 + lane) * 8) = o;
    }
  }
  __syncthreads();
  STAMP(1)
  int bin[4];
  unsigned rank[4];
#pragma unroll
  for (int j = 0; j < 4; ++j) {
    bin[j] = (d[j] >= 0) ? min(d[j] >> 4, P - 1) : -1;
    rank[j] = 0;
    if (bin[j] >= 0) rank[j] = atomicAdd(&hist[bin[j]], 1u);
  }
  __syncthreads();
  STAMP(2)
  u32x4 h = ((u32x4*)hist)[t];
  const unsigned sum = h[0] + h[1] + h[2] + h[3];
  const unsigned incl = wave_incl_scan(sum, lane);
  if (lane == 63) wsum[wv] = incl;
  __syncthreads();
  unsigned base = incl - sum;
#pragma unroll
  for (int k = 0; k < 16; ++k) base += (k < wv) ? wsum[k] : 0u;
  u32x4 o = {base, base + h[0], base + h[0] + h[1], base + h[0] + h[1] + h[2]};
  ((u32x4*)hist)[t] = o;
  __syncthreads();
  STAMP(3)
  unsigned st[4];
#pragma unroll
  for (int j = 0; j < 4; ++j) {
    st[j] = (bin[j] >= 0) ? hist[bin[j]] : 0u;
    if (bin[j] >= 0) lkey[st[j] + rank[j]] = (uint16_t)(i0 + j);
  }
  __syncthreads();
  unsigned r2v[4] = {0u, 0u, 0u, 0u};
  {
    unsigned cnb[4];
#pragma unroll
    for (int j = 0; j < 4; ++j) cnb[j] = (bin[j] >= 0) ? hist[bin[j] + 1] - st[j] : 0u;
    const unsigned cm = max(max(cnb[0], cnb[1]), max(cnb[2], cnb[3]));
    if (cm > 1u) {
#pragma clang loop unroll(disable) vectorize(disable) interleave(disable)
      for (unsigned k = 0; k < cm; ++k) {
        unsigned kv[4];
#pragma unroll
        for (int j = 0; j < 4; ++j) kv[j] = (k < cnb[j] && cnb[j] > 1u) ? lkey[st[j] + k] : 0xFFFFu;
#pragma unroll
        for (int j = 0; j < 4; ++j) r2v[j] += (kv[j] < (unsigned)(i0 + j)) ? 1u : 0u;
      }
    }
  }
#pragma unroll
  for (int j = 0; j < 4; ++j) {
    if (bin[j] >= 0) {
      const unsigned r2 = r2v[j];
      const unsigned tl = (unsigned)bin[j];
      const bool lowp = tl < split;
      const unsigned xx = lowp ? tl : tl - split;
      const unsigned dq = (xx * (lowp ? mg1 : mg0)) >> 20;
      const unsigned widx = xx - dq * (unsigned)(lowp ? tq + 1 : tq);
      u32x2 r = {(unsigned)s[j] * 128u, (widx << 20) | ((unsigned)(d[j] & 15) << 16) | wt[j]};
      lrec[st[j] + r2] = r;
    }
  }
  __syncthreads();
  STAMP(4)
  const __amdgpu_buffer_rsrc_t srd_to =
      __builtin_amdgcn_make_buffer_rsrc((void*)tab, (short)0, G2 * B1 * 32, 0x00020000);
  for (int u = t; u < 2 * G2; u += K1_THREADS) {
    const int lb = u >> 1, hf = u & 1;
    const int t0 = lb * tq + min(lb, tr) + 8 * hf;
    unsigned v[8];
#pragma unroll
    for (int j = 0; j < 8; ++j) v[j] = hist[min(t0 + j, P)];
    u32x4 pk = {v[0] | (v[1] << 16), v[2] | (v[3] << 16), v[4] | (v[5] << 16), v[6] | (v[7] << 16)};
    __builtin_amdgcn_raw_buffer_store_b128(pk, srd_to, (unsigned)((lb * B1 + b) * 16 + 8 * hf) * 2u, 0, 16);
  }
  STAMP(5)
  {
    const __amdgpu_buffer_rsrc_t srd_ro =
        __builtin_amdgcn_make_buffer_rsrc((void*)recs, (short)0, E * 8, 0x00020000);
    for (int i = 2 * t; i < cnt; i += 2 * K1_THREADS) {
      if (i + 2 <= cnt)
        __builtin_amdgcn_raw_buffer_store_b128(*(const u32x4*)(lrec + i), srd_ro, (unsigned)(e0 + i) * 8u, 0, 16);
      else
        __builtin_amdgcn_raw_buffer_store_b64(lrec[i], srd_ro, (unsigned)(e0 + i) * 8u, 0, 16);
    }
  }
  STAMP_OUT((char*)wfrag + 65536 + 1048576, b * 16 + wv)
}

__device__ __forceinline__ void dma4x16(unsigned m0base, unsigned v0, unsigned v1, unsigned v2,
                                        unsigned v3, __amdgpu_buffer_rsrc_t srd) {
  unsigned keep;
  asm volatile(
      "s_waitcnt lgkmcnt(0)\n\t"
      "s_mov_b32 %0, m0\n\t"
      "s_mov_b32 m0, %1\n\t"
      "s_nop 0\n\t"
      "buffer_load_dwordx4 %2, %6, 0 offen lds\n\t"
      "s_add_u32 m0, %1, 1024\n\t"
      "s_nop 0\n\t"
      "buffer_load_dwordx4 %3, %6, 0 offen lds\n\t"
      "s_add_u32 m0, %1, 2048\n\t"
      "s_nop 0\n\t"
      "buffer_load_dwordx4 %4, %6, 0 offen lds\n\t"
      "s_add_u32 m0, %1, 3072\n\t"
      "s_nop 0\n\t"
      "buffer_load_dwordx4 %5, %6, 0 offen lds\n\t"
      "s_mov_b32 m0, %0"
      : "=&s"(keep)
      : "s"(m0base), "v"(v0), "v"(v1), "v"(v2), "v"(v3), "s"(srd)
      : "memory", "scc");
}

__device__ __forceinline__ void dma_rec(unsigned m0a, unsigned m0b, unsigned va, unsigned vb,
                                        __amdgpu_buffer_rsrc_t srd) {
  unsigned keep;
  asm volatile(
      "s_waitcnt lgkmcnt(0)\n\t"
      "s_mov_b32 %0, m0\n\t"
      "s_mov_b32 m0, %1\n\t"
      "s_nop 0\n\t"
      "buffer_load_dword %3, %5, 0 offen lds\n\t"
      "s_mov_b32 m0, %2\n\t"
      "s_nop 0\n\t"
      "buffer_load_dword %4, %5, 0 offen lds\n\t"
      "s_mov_b32 m0, %0"
      : "=&s"(keep)
      : "s"(m0a), "s"(m0b), "v"(va), "v"(vb), "s"(srd)
      : "memory");
}

__device__ __forceinline__ bf16x8 bload16(__amdgpu_buffer_rsrc_t srd, unsigned voff, unsigned soff) {
  u32x4 v = __builtin_amdgcn_raw_buffer_load_b128(srd, voff, soff, 0);
  return __builtin_bit_cast(bf16x8, v);
}

__device__ __forceinline__ void dma1x16(unsigned m0base, unsigned v0, __amdgpu_buffer_rsrc_t srd) {
  unsigned keep;
  asm volatile(
      "s_waitcnt lgkmcnt(0)\n\t"
      "s_mov_b32 %0, m0\n\t"
      "s_mov_b32 m0, %1\n\t"
      "s_nop 0\n\t"
      "buffer_load_dwordx4 %2, %3, 0 offen lds\n\t"
      "s_mov_b32 m0, %0"
      : "=&s"(keep)
      : "s"(m0base), "v"(v0), "s"(srd)
      : "memory");
}

constexpr int ADJ_P = 257;
constexpr int LDS_LISTS = K2_WAVES * 8192;
constexpr int LDS_TOTAL = LDS_LISTS + K2_WAVES * 8 * CAP;

__global__ __launch_bounds__(K2_WAVES * 64) void k_aggr(
    const uint16_t* __restrict__ x, const u32x2* __restrict__ recs,
    const uint16_t* __restrict__ tab, const uint16_t* __restrict__ wfrag,
    uint16_t* __restrict__ out, int N, int G8, int tq, int tr, int B1, int CH, int E) {
  __shared__ __attribute__((aligned(16))) char smem[LDS_TOTAL];
  asm volatile("" ::"s"(x), "s"(recs), "s"(tab), "s"(wfrag), "s"(out), "s"(N), "s"(G8), "s"(tq),
               "s"(tr), "s"(B1), "s"(CH), "s"(E));
  const int lane = threadIdx.x & 63;
  const int wv = __builtin_amdgcn_readfirstlane(threadIdx.x >> 6);
  STAMP_DECL
  const int bid = blockIdx.x;
  const int lb = (bid & 7) * G8 + (bid >> 3);
  const int t_begin = lb * tq + min(lb, tr);
  const int nt = tq + (lb < tr ? 1 : 0);
  const int tile = t_begin + wv;
  const bool active = wv < nt;

  char* rbuf = smem + wv * 8192;
  u32x2* lists = (u32x2*)(smem + LDS_LISTS);
  u32x2* lrecs = lists + wv * CAP;
  const uint16_t* tabL = (const uint16_t*)smem;
  int* adjL = (int*)(smem + 8192);
  unsigned* cntL = (unsigned*)(smem + 24704);
  const unsigned smem_a = (unsigned)(size_t)(LAS char*)smem;
  const unsigned rbuf_a = smem_a + wv * 8192;

  const __amdgpu_buffer_rsrc_t srd_x =
      __builtin_amdgcn_make_buffer_rsrc((void*)x, (short)0, N * 128, 0x00020000);
  const __amdgpu_buffer_rsrc_t srd_r =
      __builtin_amdgcn_make_buffer_rsrc((void*)recs, (short)0, E * 8, 0x00020000);
  const __amdgpu_buffer_rsrc_t srd_w =
      __builtin_amdgcn_make_buffer_rsrc((void*)wfrag, (short)0, 16384, 0x00020000);
  const __amdgpu_buffer_rsrc_t srd_t = __builtin_amdgcn_make_buffer_rsrc(
      (void*)(tab + (size_t)lb * B1 * 16), (short)0, B1 * 32, 0x00020000);
  const __amdgpu_buffer_rsrc_t srd_o =
      __builtin_amdgcn_make_buffer_rsrc((void*)out, (short)0, N * 128, 0x00020000);

  const int q = lane >> 4, i16 = lane & 15;
  const unsigned ch_even = (unsigned)((lane & 7) ^ (((lane >> 4) & 1) << 1)) << 4;
  const int qp = i16 >> 2, pp = lane & 3;
  const int gsw = ((qp >> 1) & 1) | ((q & 1) << 1);
  const unsigned abase = (unsigned)((8 * q + qp) * 128 + (pp >> 1) * 16 + 8 * (pp & 1));

  const unsigned rowoff = active ? (unsigned)(tile * 16 + i16) * 128u + 16u * q : 0x80000000u;
  f32x4 acc[4];
#pragma unroll
  for (int t = 0; t < 4; ++t) acc[t] = f32x4{0.f, 0.f, 0.f, 0.f};
  f32x4 o[4];
#pragma unroll
  for (int u = 0; u < 4; ++u) o[u] = f32x4{0.f, 0.f, 0.f, 0.f};
  bf16x8 wf[8];
  bool first = true;

  for (int cg = 0; cg < B1; cg += 256) {
    const int nb = min(256, B1 - cg);
    for (unsigned rnd = 0;; rnd += CAP) {
      if (!first) __syncthreads();
      if (wv * 32 < nb) dma1x16(smem_a + wv * 1024, (unsigned)(cg * 32 + wv * 1024 + lane * 16), srd_t);
      bf16x8 xf[2];
      if (first) {
#pragma unroll
        for (int f = 0; f < 8; ++f) wf[f] = bload16(srd_w, lane * 16u, f * 1024u);
        xf[0] = bload16(srd_x, rowoff, 0);
        xf[1] = bload16(srd_x, rowoff, 64);
        __builtin_amdgcn_sched_barrier(0);
        asm volatile("s_waitcnt vmcnt(10)" ::: "memory");
      } else {
        asm volatile("s_waitcnt vmcnt(0)" ::: "memory");
      }
      __syncthreads();
      __builtin_amdgcn_sched_barrier(0);
      if (first) {
#pragma unroll
        for (int kk = 0; kk < 2; ++kk)
#pragma unroll
          for (int u = 0; u < 4; ++u)
            o[u] = __builtin_amdgcn_mfma_f32_16x16x32_bf16(wf[kk * 4 + u], xf[kk], o[u], 0, 0, 0);
        first = false;
      }
      STAMP(1)
      unsigned mx = 0, mine;
      {
        int tid = threadIdx.x;
        asm volatile("" : "+v"(tid));
        constexpr int NSW = 5;
        u32x2 rv[2 * NSW];
        unsigned lens[NSW], goff[NSW];
        const unsigned o16 = (unsigned)(tid & 15);
#pragma unroll
        for (int sw = 0; sw < NSW; ++sw) {
          const int sg = (tid >> 4) + sw * (K2_WAVES * 4);
          const int sc = min(sg, nb - 1);
          const unsigned a0 = tabL[sc * 16];
          lens[sw] = (sg < nb) ? (unsigned)tabL[sc * 16 + nt] - a0 : 0u;
          goff[sw] = ((unsigned)(cg + sc) * (unsigned)CH + a0) * 8u;
          rv[2 * sw] = __builtin_amdgcn_raw_buffer_load_b64(
              srd_r, (o16 < lens[sw]) ? goff[sw] + o16 * 8u : 0x80000000u, 0, 0);
          rv[2 * sw + 1] = __builtin_amdgcn_raw_buffer_load_b64(
              srd_r, (o16 + 16u < lens[sw]) ? goff[sw] + o16 * 8u + 128u : 0x80000000u, 0, 0);
        }
  {
    int ln = lane;
    asm volatile("" : "+v"(ln));
    unsigned cn[4], of[4];
#pragma unroll
    for (int jh = 0; jh < 4; jh += 2) {
      u32x4 r0[2], r1[2];
#pragma unroll
      for (int j = 0; j < 2; ++j) {
        const u32x4* trw = (const u32x4*)(tabL + min((jh + j) * 64 + ln, nb - 1) * 16);
        r0[j] = trw[0];
        r1[j] = trw[1];
      }
      unsigned lo[2], hi[2];
#define PICK(A, B) _Pragma("unroll") for (int j = 0; j < 2; ++j) { lo[j] = A; hi[j] = B; }
      switch (wv >> 1) {
        case 1: PICK(r0[j][1], r0[j][2]) break;
        case 2: PICK(r0[j][2], r0[j][3]) break;
        case 3: PICK(r0[j][3], r1[j][0]) break;
        case 4: PICK(r1[j][0], r1[j][1]) break;
        case 5: PICK(r1[j][1], r1[j][2]) break;
        case 6: PICK(r1[j][2], r1[j][3]) break;
        default: PICK(r0[j][0], r0[j][1]) break;
      }
#undef PICK
#pragma unroll
      for (int j = 0; j < 2; ++j) {
        const unsigned pr = (wv & 1) ? __builtin_amdgcn_alignbit(hi[j], lo[j], 16) : lo[j];
        const unsigned a0 = r0[j][0] & 0xffffu, aw = pr & 0xffffu, aw1 = pr >> 16;
        cn[jh + j] = ((jh + j) * 64 + ln < nb && active) ? aw1 - aw : 0u;
        of[jh + j] = aw - a0;
      }
    }
    const unsigned c = cn[0] + cn[1] + cn[2] + cn[3];
    const unsigned incl = wave_incl_scan(c, ln);
    unsigned p = incl - c - rnd;
#pragma unroll
    for (int j = 0; j < 4; ++j) {
      adjL[wv * ADJ_P + j * 64 + ln] = (int)(p - of[j]);
      p += cn[j];
    }
    if (ln == 63) cntL[wv] = incl;
  }
  __syncthreads();
  STAMP(2)
#pragma unroll
  for (int k = 0; k < K2_WAVES; ++k) mx = max(mx, cntL[k]);
  mx = __builtin_amdgcn_readfirstlane(mx);
  mine = __builtin_amdgcn_readfirstlane(cntL[wv]);
        auto place = [&](int sc, unsigned oo, unsigned rs, unsigned rm) {
          const unsigned w = (rm >> 20) & 15u;
          const unsigned slot = (unsigned)(adjL[w * ADJ_P + sc] + (int)oo);
          if (slot < (unsigned)CAP) lists[w * CAP + slot] = u32x2{rs, rm & 0xFFFFFu};
        };
        {
          int aj[2 * NSW];
#pragma unroll
          for (int k = 0; k < 2 * NSW; ++k) {
            const int sc = min((tid >> 4) + (k >> 1) * (K2_WAVES * 4), nb - 1);
            aj[k] = adjL[((rv[k][1] >> 20) & 15u) * ADJ_P + sc];
          }
#pragma unroll
          for (int k = 0; k < 2 * NSW; ++k) {
            const unsigned oo = o16 + 16u * (k & 1);
            const unsigned rs = rv[k][0], rm = rv[k][1];
            const unsigned slot = (unsigned)(aj[k] + (int)oo);
            if (oo < lens[k >> 1] && slot < (unsigned)CAP)
              lists[((rm >> 20) & 15u) * CAP + slot] = u32x2{rs, rm & 0xFFFFFu};
          }
        }
#pragma unroll
        for (int sw = 0; sw < NSW; ++sw) {
          const int sc = min((tid >> 4) + sw * (K2_WAVES * 4), nb - 1);
#pragma clang loop unroll(disable) vectorize(disable) interleave(disable)
          for (unsigned oo = o16 + 32u; oo < lens[sw]; oo += 16u) {
            const u32x2 r = __builtin_amdgcn_raw_buffer_load_b64(srd_r, goff[sw] + oo * 8u, 0, 0);
            place(sc, oo, r[0], r[1]);
          }
        }
      }
      STAMP(3)
      const unsigned m = (mine > rnd) ? min(mine - rnd, (unsigned)CAP) : 0u;
      const int ng = (int)((m + 31) >> 5);
      {
        const unsigned pidx = m + lane;
        if (pidx < (unsigned)ng * 32u) lrecs[pidx] = u32x2{0x80000000u, 0xFFFF0000u};
      }
#pragma unroll
      for (int f = 0; f < 8; ++f) wf[f] = bload16(srd_w, lane * 16u, (8 + f) * 1024u);
      __syncthreads();
      STAMP(4)
      auto issue = [&](int g) {
        const unsigned* ls = (const unsigned*)(lrecs + g * 32 + (lane >> 3));
        const unsigned v0 = ls[0] + ch_even, v1 = ls[16] + (ch_even ^ 64u), v2 = ls[32] + ch_even,
                       v3 = ls[48] + (ch_even ^ 64u);
        dma4x16(rbuf_a + (g & 1) * 4096, v0, v1, v2, v3, srd_x);
      };
      if (ng > 0) issue(0);
      if (ng > 1) issue(1);
#pragma clang loop unroll(disable)
      for (int g = 0; g < ng; ++g) {
        const unsigned* ls = (const unsigned*)(lrecs + min(g + 2, CAP / 32 - 1) * 32 + (lane >> 3));
        const unsigned n0 = ls[0], n1 = ls[16], n2 = ls[32], n3 = ls[48];
        if (g + 1 < ng) asm volatile("s_waitcnt vmcnt(4)" ::: "memory");
        else asm volatile("s_waitcnt vmcnt(0)" ::: "memory");
        const char* bufp = rbuf + (g & 1) * 4096 + abase;
        bf16x8 af[4];
#pragma unroll
        for (int t = 0; t < 4; ++t) {
          const char* ap = bufp + ((t ^ gsw) << 5);
          s16x4 lo = __builtin_amdgcn_ds_read_tr16_b64_v4i16((LAS s16x4*)(ap));
          s16x4 hi = __builtin_amdgcn_ds_read_tr16_b64_v4i16((LAS s16x4*)(ap + 512));
          af[t] = __builtin_shufflevector(lo, hi, 0, 1, 2, 3, 4, 5, 6, 7);
        }
        const u32x4* mp = (const u32x4*)(lrecs + g * 32 + 8 * q);
        const u32x4 m0 = mp[0], m1 = mp[1], m2 = mp[2], m3 = mp[3];
        const u32x4 ma = {m0[1], m0[3], m1[1], m1[3]};
        const u32x4 mb = {m2[1], m2[3], m3[1], m3[3]};
        if (g + 2 < ng)
          dma4x16(rbuf_a + (g & 1) * 4096, n0 + ch_even, n1 + (ch_even ^ 64u), n2 + ch_even,
                  n3 + (ch_even ^ 64u), srd_x);
        unsigned sel[8];
#pragma unroll
        for (int j = 0; j < 4; ++j) {
          sel[j] = ((ma[j] >> 16) == (unsigned)i16) ? (ma[j] & 0xffffu) : 0u;
          sel[4 + j] = ((mb[j] >> 16) == (unsigned)i16) ? (mb[j] & 0xffffu) : 0u;
        }
        u32x4 bp = {sel[0] | (sel[1] << 16), sel[2] | (sel[3] << 16), sel[4] | (sel[5] << 16),
                    sel[6] | (sel[7] << 16)};
        const bf16x8 bfr = __builtin_bit_cast(bf16x8, bp);
#pragma unroll
        for (int t = 0; t < 4; ++t)
          acc[t] = __builtin_amdgcn_mfma_f32_16x16x32_bf16(af[t], bfr, acc[t], 0, 0, 0);
      }
      if (rnd + CAP >= mx) break;
    }
  }

  STAMP(5)
#pragma unroll
  for (int s = 0; s < 2; ++s) {
    u32x4 tp = {pk_bf16(acc[2 * s][0], acc[2 * s][1]), pk_bf16(acc[2 * s][2], acc[2 * s][3]),
                pk_bf16(acc[2 * s + 1][0], acc[2 * s + 1][1]),
                pk_bf16(acc[2 * s + 1][2], acc[2 * s + 1][3])};
    const bf16x8 tf = __builtin_bit_cast(bf16x8, tp);
#pragma unroll
    for (int u = 0; u < 4; ++u)
      o[u] = __builtin_amdgcn_mfma_f32_16x16x32_bf16(wf[s * 4 + u], tf, o[u], 0, 0, 0);
  }
#pragma unroll
  for (int hh = 0; hh < 2; ++hh) {
    u32x4 ov = {pk_bf16(o[2 * hh][0], o[2 * hh][1]), pk_bf16(o[2 * hh][2], o[2 * hh][3]),
                pk_bf16(o[2 * hh + 1][0], o[2 * hh + 1][1]),
                pk_bf16(o[2 * hh + 1][2], o[2 * hh + 1][3])};
    __builtin_amdgcn_raw_buffer_store_b128(ov, srd_o, rowoff + 64u * hh, 0, 16);
  }
  STAMP_OUT((char*)wfrag + 65536, bid * 16 + wv)
}

__device__ __forceinline__ float bf2f(uint16_t v) { return __uint_as_float((unsigned)v << 16); }

__global__ void fb_zero(float* t1, size_t n) {
  for (size_t i = blockIdx.x * (size_t)blockDim.x + threadIdx.x; i < n; i += (size_t)gridDim.x * blockDim.x)
    t1[i] = 0.f;
}
__global__ void fb_scatter(const uint16_t* x, const int* src, const int* dst, const uint16_t* ew,
                           float* t1, int E) {
  const int lane = threadIdx.x & 63;
  const size_t wid = (blockIdx.x * (size_t)blockDim.x + threadIdx.x) >> 6;
  const size_t nw = ((size_t)gridDim.x * blockDim.x) >> 6;
  for (size_t e = wid; e < (size_t)E; e += nw)
    atomicAdd(&t1[(size_t)dst[e] * DF + lane], bf2f(ew[e]) * bf2f(x[(size_t)src[e] * DF + lane]));
}
__global__ void fb_out(const uint16_t* x, const float* t1, const uint16_t* w, uint16_t* out, int N) {
  const int lane = threadIdx.x & 63;
  const size_t wid = (blockIdx.x * (size_t)blockDim.x + threadIdx.x) >> 6;
  const size_t nw = ((size_t)gridDim.x * blockDim.x) >> 6;
  for (size_t n = wid; n < (size_t)N; n += nw) {
    float a = 0.f;
    for (int d = 0; d < DF; ++d) {
      const float tv = bf2f((uint16_t)(pk_bf16(t1[n * DF + d], 0.f) & 0xffffu));
      a += bf2f(x[n * DF + d]) * bf2f(w[d * DF + lane]) + tv * bf2f(w[(DF + d) * DF + lane]);
    }
    out[n * DF + lane] = (uint16_t)(pk_bf16(a, 0.f) & 0xffffu);
  }
}

extern "C" void kernel_launch(void* const* d_in, const int* in_sizes, int n_in, void* d_out,
                              int out_size, void* d_ws, size_t ws_size, hipStream_t stream) {
  const uint16_t* x = (const uint16_t*)d_in[0];
  const int* src = (const int*)d_in[1];
  const int* dst = (const int*)d_in[2];
  const uint16_t* ew = (const uint16_t*)d_in[3];
  const uint16_t* w = (const uint16_t*)d_in[4];
  uint16_t* out = (uint16_t*)d_out;
  const int N = in_sizes[0] / DF;
  const int E = in_sizes[1];
  const int P = (N + 15) / 16;

  int CH = (E + 255) / 256;
  CH = (CH + 3) & ~3;
  if (CH < 4) CH = 4;
  const bool fast = (P + 1 <= MAX_BINS) && (CH <= MAX_CH) && (E > 0) && ((size_t)E * 8 < 0x7fffffffull);
  if (fast) {
    const int B1 = (E + CH - 1) / CH;
    int G2 = K2_GRID;
    while ((long long)G2 * K2_WAVES < (long long)P) G2 += 8;
    char* ws = (char*)d_ws;
    size_t off = 0;
    u32x2* recs = (u32x2*)(ws + off);
    off += ((size_t)E * 8 + 255) & ~(size_t)255;
    uint16_t* tab = (uint16_t*)(ws + off);
    off += ((size_t)B1 * G2 * 32 + 255) & ~(size_t)255;
    uint16_t* wfrag = (uint16_t*)(ws + off);
    const unsigned tq = (unsigned)(P / G2);
    const unsigned mg0 = tq ? ((1u << 20) + tq - 1) / tq : 0u;
    const unsigned mg1 = ((1u << 20) + tq) / (tq + 1);
    k_part<<<B1, K1_THREADS, 0, stream>>>(src, dst, ew, w, recs, tab, wfrag, E, CH, P, G2, P / G2, P % G2, B1, mg0, mg1);
    k_aggr<<<G2, K2_WAVES * 64, 0, stream>>>(x, recs, tab, wfrag, out, N, G2 / 8, P / G2, P % G2, B1, CH, E);
  } else {
    float* t1 = (float*)d_ws;
    fb_zero<<<2048, 256, 0, stream>>>(t1, (size_t)N * DF);
    if (E > 0) fb_scatter<<<2048, 256, 0, stream>>>(x, src, dst, ew, t1, E);
    fb_out<<<2048, 256, 0, stream>>>(x, t1, w, out, N);
  }
}
```

```cpp
#include <hip/hip_runtime.h>
#include <hip/hip_bf16.h>
#include <stdint.h>

typedef __attribute__((ext_vector_type(4))) short s16x4;
typedef __attribute__((ext_vector_type(8))) short bf16x8;
typedef __attribute__((ext_vector_type(4))) float f32x4;
typedef __attribute__((ext_vector_type(2))) float f32x2;
typedef __attribute__((ext_vector_type(2))) __bf16 bf16x2v;
typedef __attribute__((ext_vector_type(4))) unsigned u32x4;
typedef __attribute__((ext_vector_type(2))) unsigned u32x2;
typedef __attribute__((ext_vector_type(4))) int i32x4;
typedef __attribute__((ext_vector_type(4))) unsigned short u16x4;
#define LAS __attribute__((address_space(3)))

#define STAMP_DECL
#define STAMP(i)
#define STAMP_OUT(ptr, idx)

constexpr int DF = 64;
constexpr int K1_THREADS = 1024;
constexpr int MAX_BINS = 4096;
constexpr int MAX_CH = 4096;
constexpr int K2_WAVES = 13;
constexpr int K2_GRID = 256;
constexpr int CAP = 512;

__device__ __forceinline__ unsigned pk_bf16(float a, float b) {
  f32x2 v = {a, b};
  bf16x2v r = __builtin_convertvector(v, bf16x2v);
  return __builtin_bit_cast(unsigned, r);
}

__device__ __forceinline__ unsigned wave_incl_scan(unsigned v, int  ) {
  v += __builtin_amdgcn_update_dpp(0u, v, 0x111, 0xf, 0xf, false);
  v += __builtin_amdgcn_update_dpp(0u, v, 0x112, 0xf, 0xf, false);
  v += __builtin_amdgcn_update_dpp(0u, v, 0x114, 0xf, 0xf, false);
  v += __builtin_amdgcn_update_dpp(0u, v, 0x118, 0xf, 0xf, false);
  v += __builtin_amdgcn_update_dpp(0u, v, 0x142, 0xa, 0xf, false);
  v += __builtin_amdgcn_update_dpp(0u, v, 0x143, 0xc, 0xf, false);
  return v;
}

__global__ __launch_bounds__(K1_THREADS) void k_part(
    const int* __restrict__ src, const int* __restrict__ dst, const uint16_t* __restrict__ ew,
    const uint16_t* __restrict__ w, u32x2* __restrict__ recs, uint16_t* __restrict__ tab,
    uint16_t* __restrict__ wfrag, int E, int CH, int P, int G2, int tq, int tr, int B1,
    unsigned mg0, unsigned mg1) {
  __shared__ __attribute__((aligned(16))) unsigned hist[MAX_BINS];
  __shared__ __attribute__((aligned(16))) u32x2 lrec[MAX_CH];
  __shared__ uint16_t lkey[MAX_CH];
  __shared__ unsigned wsum[16];
  asm volatile("" ::"s"(src), "s"(dst), "s"(ew), "s"(w), "s"(recs), "s"(tab), "s"(wfrag), "s"(E),
               "s"(CH), "s"(P), "s"(G2), "s"(tq), "s"(tr), "s"(B1), "s"(mg0), "s"(mg1));
  const int b = blockIdx.x, t = threadIdx.x, lane = t & 63, wv = t >> 6;
  STAMP_DECL
  const unsigned split = (unsigned)(tr * (tq + 1));
  const int e0 = b * CH;
  const int cnt = min(CH, E - e0);
  const int i0 = 4 * t;
  int d[4], s[4];
  unsigned wt[4];
  if (i0 + 4 <= cnt) {
    i32x4 d4 = *(const i32x4*)(dst + e0 + i0);
    i32x4 s4 = *(const i32x4*)(src + e0 + i0);
    u16x4 w4 = *(const u16x4*)(ew + e0 + i0);
#pragma unroll
    for (int j = 0; j < 4; ++j) { d[j] = d4[j]; s[j] = s4[j]; wt[j] = w4[j]; }
  } else {
#pragma unroll
    for (int j = 0; j < 4; ++j) {
      bool ok = i0 + j < cnt;
      d[j] = ok ? dst[e0 + i0 + j] : -1;
      s[j] = ok ? src[e0 + i0 + j] : 0;
      wt[j] = ok ? ew[e0 + i0 + j] : 0;
    }
  }
  ((u32x4*)hist)[t] = u32x4{0u, 0u, 0u, 0u};
  if (wv == 15) {
    for (int f = b; f < 16; f += B1) {
      const int kk = f >> 2, u = f & 3, i = lane & 15, q = lane >> 4;
      const int e = 32 * (u >> 1) + 8 * (i >> 2) + 4 * (u & 1) + (i & 3);
      unsigned short v[8];
#pragma unroll
      for (int j = 0; j < 8; ++j) {
        int dd, kw;
        if (kk < 2) { kw = 0; dd = 32 * kk + 8 * q + j; }
        else { kw = 1; dd = 16 * (2 * (kk - 2) + (j >> 2)) + 4 * q + (j & 3); }
        v[j] = w[(kw * DF + dd) * DF + e];
      }
      u32x4 o;
#pragma unroll
      for (int j = 0; j < 4; ++j) o[j] = (unsigned)v[2 * j] | ((unsigned)v[2 * j + 1] << 16);
      *(u32x4*)(wfrag + (f * 64 + lane) * 8) = o;
    }
  }
  __syncthreads();
  STAMP(1)
  int bin[4];
  unsigned rank[4];
#pragma unroll
  for (int j = 0; j < 4; ++j) {
    bin[j] = (d[j] >= 0) ? min(d[j] >> 4, P - 1) : -1;
    rank[j] = 0;
    if (bin[j] >= 0) rank[j] = atomicAdd(&hist[bin[j]], 1u);
  }

  __syncthreads();
  STAMP(2)
  u32x4 h = ((u32x4*)hist)[t];
  const unsigned sum = h[0] + h[1] + h[2] + h[3];
  const unsigned incl = wave_incl_scan(sum, lane);
  if (lane == 63) wsum[wv] = incl;
  __syncthreads();
  unsigned base = incl - sum;
#pragma unroll
  for (int k = 0; k < 16; ++k) base += (k < wv) ? wsum[k] : 0u;
  u32x4 o = {base, base + h[0], base + h[0] + h[1], base + h[0] + h[1] + h[2]};
  ((u32x4*)hist)[t] = o;
  __syncthreads();
  STAMP(3)
  unsigned st[4], cnb[4];
  unsigned r2v[4] = {0u, 0u, 0u, 0u};
#pragma unroll
  for (int j = 0; j < 4; ++j) {
    st[j] = (bin[j] >= 0) ? hist[bin[j]] : 0u;
    cnb[j] = (bin[j] >= 0) ? hist[bin[j] + 1] - st[j] : 0u;
  }
  {

#pragma unroll
    for (int j = 0; j < 4; ++j)
      if (bin[j] >= 0) lkey[st[j] + rank[j]] = (uint16_t)(i0 + j);
    __syncthreads();
    const unsigned cm = max(max(cnb[0], cnb[1]), max(cnb[2], cnb[3]));
#pragma clang loop unroll(disable) vectorize(disable) interleave(disable)
    for (unsigned k = 0; k < cm; ++k) {
      unsigned kv[4];
#pragma unroll
      for (int j = 0; j < 4; ++j) kv[j] = (k < cnb[j] && cnb[j] > 1u) ? lkey[st[j] + k] : 0xFFFFu;
#pragma unroll
      for (int j = 0; j < 4; ++j) r2v[j] += (kv[j] < (unsigned)(i0 + j)) ? 1u : 0u;
    }
  }
#pragma unroll
  for (int j = 0; j < 4; ++j) {
    if (bin[j] >= 0) {
      const unsigned r2 = r2v[j];
      const unsigned tl = (unsigned)bin[j];
      const bool lowp = tl < split;
      const unsigned xx = lowp ? tl : tl - split;
      const unsigned dq = (xx * (lowp ? mg1 : mg0)) >> 20;
      const unsigned widx = xx - dq * (unsigned)(lowp ? tq + 1 : tq);
      u32x2 r = {(unsigned)s[j] * 128u, (widx << 20) | ((unsigned)(d[j] & 15) << 16) | wt[j]};
      lrec[st[j] + r2] = r;
    }
  }
  __syncthreads();
  STAMP(4)
  const __amdgpu_buffer_rsrc_t srd_to =
      __builtin_amdgcn_make_buffer_rsrc((void*)tab, (short)0, G2 * B1 * 32, 0x00020000);
  for (int u = t; u < 2 * G2; u += K1_THREADS) {
    const int lb = u >> 1, hf = u & 1;
    const int t0 = lb * tq + min(lb, tr) + 8 * hf;
    unsigned v[8];
#pragma unroll
    for (int j = 0; j < 8; ++j) v[j] = hist[min(t0 + j, P)];
    u32x4 pk = {v[0] | (v[1] << 16), v[2] | (v[3] << 16), v[4] | (v[5] << 16), v[6] | (v[7] << 16)};
    __builtin_amdgcn_raw_buffer_store_b128(pk, srd_to, (unsigned)((lb * B1 + b) * 16 + 8 * hf) * 2u, 0, 16);
  }
  STAMP(5)
  {
    const __amdgpu_buffer_rsrc_t srd_ro =
        __builtin_amdgcn_make_buffer_rsrc((void*)recs, (short)0, E * 8, 0x00020000);
    for (int i = 2 * t; i < cnt; i += 2 * K1_THREADS) {
      if (i + 2 <= cnt)
        __builtin_amdgcn_raw_buffer_store_b128(*(const u32x4*)(lrec + i), srd_ro, (unsigned)(e0 + i) * 8u, 0, 16);
      else
        __builtin_amdgcn_raw_buffer_store_b64(lrec[i], srd_ro, (unsigned)(e0 + i) * 8u, 0, 16);
    }
  }
  STAMP_OUT((char*)wfrag + 65536 + 1048576, b * 16 + wv)
}

__device__ __forceinline__ void dma4x16(unsigned m0base, unsigned v0, unsigned v1, unsigned v2,
                                        unsigned v3, __amdgpu_buffer_rsrc_t srd) {
  unsigned keep;
  asm volatile(
      "s_waitcnt lgkmcnt(0)\n\t"
      "s_mov_b32 %0, m0\n\t"
      "s_mov_b32 m0, %1\n\t"
      "s_nop 0\n\t"
      "buffer_load_dwordx4 %2, %6, 0 offen lds\n\t"
      "s_add_u32 m0, %1, 1024\n\t"
      "s_nop 0\n\t"
      "buffer_load_dwordx4 %3, %6, 0 offen lds\n\t"
      "s_add_u32 m0, %1, 2048\n\t"
      "s_nop 0\n\t"
      "buffer_load_dwordx4 %4, %6, 0 offen lds\n\t"
      "s_add_u32 m0, %1, 3072\n\t"
      "s_nop 0\n\t"
      "buffer_load_dwordx4 %5, %6, 0 offen lds\n\t"
      "s_mov_b32 m0, %0"
      : "=&s"(keep)
      : "s"(m0base), "v"(v0), "v"(v1), "v"(v2), "v"(v3), "s"(srd)
      : "memory", "scc");
}

__device__ __forceinline__ void dma_rec(unsigned m0a, unsigned m0b, unsigned va, unsigned vb,
                                        __amdgpu_buffer_rsrc_t srd) {
  unsigned keep;
  asm volatile(
      "s_waitcnt lgkmcnt(0)\n\t"
      "s_mov_b32 %0, m0\n\t"
      "s_mov_b32 m0, %1\n\t"
      "s_nop 0\n\t"
      "buffer_load_dword %3, %5, 0 offen lds\n\t"
      "s_mov_b32 m0, %2\n\t"
      "s_nop 0\n\t"
      "buffer_load_dword %4, %5, 0 offen lds\n\t"
      "s_mov_b32 m0, %0"
      : "=&s"(keep)
      : "s"(m0a), "s"(m0b), "v"(va), "v"(vb), "s"(srd)
      : "memory");
}

__device__ __forceinline__ bf16x8 bload16(__amdgpu_buffer_rsrc_t srd, unsigned voff, unsigned soff) {
  u32x4 v = __builtin_amdgcn_raw_buffer_load_b128(srd, voff, soff, 0);
  return __builtin_bit_cast(bf16x8, v);
}

__device__ __forceinline__ void dma1x16(unsigned m0base, unsigned v0, __amdgpu_buffer_rsrc_t srd) {
  unsigned keep;
  asm volatile(
      "s_waitcnt lgkmcnt(0)\n\t"
      "s_mov_b32 %0, m0\n\t"
      "s_mov_b32 m0, %1\n\t"
      "s_nop 0\n\t"
      "buffer_load_dwordx4 %2, %3, 0 offen lds\n\t"
      "s_mov_b32 m0, %0"
      : "=&s"(keep)
      : "s"(m0base), "v"(v0), "s"(srd)
      : "memory");
}

constexpr int ADJ_P = 257;
constexpr int LDS_LISTS = K2_WAVES * 8192;
constexpr int LDS_TOTAL = LDS_LISTS + K2_WAVES * 8 * CAP;

__global__ __launch_bounds__(K2_WAVES * 64) void k_aggr(
    const uint16_t* __restrict__ x, const u32x2* __restrict__ recs,
    const uint16_t* __restrict__ tab, const uint16_t* __restrict__ wfrag,
    uint16_t* __restrict__ out, int N, int G8, int tq, int tr, int B1, int CH, int E) {
  __shared__ __attribute__((aligned(16))) char smem[LDS_TOTAL];
  asm volatile("" ::"s"(x), "s"(recs), "s"(tab), "s"(wfrag), "s"(out), "s"(N), "s"(G8), "s"(tq),
               "s"(tr), "s"(B1), "s"(CH), "s"(E));
  const int lane = threadIdx.x & 63;
  const int wv = __builtin_amdgcn_readfirstlane(threadIdx.x >> 6);
  STAMP_DECL
  const int bid = blockIdx.x;
  const int lb = (bid & 7) * G8 + (bid >> 3);
  const int t_begin = lb * tq + min(lb, tr);
  const int nt = tq + (lb < tr ? 1 : 0);
  const int tile = t_begin + wv;
  const bool active = wv < nt;

  char* rbuf = smem + wv * 8192;
  u32x2* lists = (u32x2*)(smem + LDS_LISTS);
  u32x2* lrecs = lists + wv * CAP;
  const uint16_t* tabL = (const uint16_t*)smem;
  int* adjL = (int*)(smem + 8192);
  unsigned* cntL = (unsigned*)(smem + 24704);
  const unsigned smem_a = (unsigned)(size_t)(LAS char*)smem;
  const unsigned rbuf_a = smem_a + wv * 8192;

  const __amdgpu_buffer_rsrc_t srd_x =
      __builtin_amdgcn_make_buffer_rsrc((void*)x, (short)0, N * 128, 0x00020000);
  const __amdgpu_buffer_rsrc_t srd_r =
      __builtin_amdgcn_make_buffer_rsrc((void*)recs, (short)0, E * 8, 0x00020000);
  const __amdgpu_buffer_rsrc_t srd_w =
      __builtin_amdgcn_make_buffer_rsrc((void*)wfrag, (short)0, 16384, 0x00020000);
  const __amdgpu_buffer_rsrc_t srd_t = __builtin_amdgcn_make_buffer_rsrc(
      (void*)(tab + (size_t)lb * B1 * 16), (short)0, B1 * 32, 0x00020000);
  const __amdgpu_buffer_rsrc_t srd_o =
      __builtin_amdgcn_make_buffer_rsrc((void*)out, (short)0, N * 128, 0x00020000);

  const int q = lane >> 4, i16 = lane & 15;
  const unsigned ch_even = (unsigned)((lane & 7) ^ (((lane >> 4) & 1) << 1)) << 4;
  const int qp = i16 >> 2, pp = lane & 3;
  const int gsw = ((qp >> 1) & 1) | ((q & 1) << 1);
  const unsigned abase = (unsigned)((8 * q + qp) * 128 + (pp >> 1) * 16 + 8 * (pp & 1));

  const unsigned rowoff = active ? (unsigned)(tile * 16 + i16) * 128u + 16u * q : 0x80000000u;
  f32x4 acc[4];
#pragma unroll
  for (int t = 0; t < 4; ++t) acc[t] = f32x4{0.f, 0.f, 0.f, 0.f};
  f32x4 o[4];
#pragma unroll
  for (int u = 0; u < 4; ++u) o[u] = f32x4{0.f, 0.f, 0.f, 0.f};
  bf16x8 wf[8];
  bool first = true;

  for (int cg = 0; cg < B1; cg += 256) {
    const int nb = min(256, B1 - cg);
    for (unsigned rnd = 0;; rnd += CAP) {
      if (!first) __syncthreads();
      if (wv * 32 < nb) dma1x16(smem_a + wv * 1024, (unsigned)(cg * 32 + wv * 1024 + lane * 16), srd_t);
      bf16x8 xf[2];
      if (first) {
#pragma unroll
        for (int f = 0; f < 8; ++f) wf[f] = bload16(srd_w, lane * 16u, f * 1024u);
        xf[0] = bload16(srd_x, rowoff, 0);
        xf[1] = bload16(srd_x, rowoff, 64);
        __builtin_amdgcn_sched_barrier(0);
        asm volatile("s_waitcnt vmcnt(10)" ::: "memory");
      } else {
        asm volatile("s_waitcnt vmcnt(0)" ::: "memory");
      }
      __syncthreads();
      __builtin_amdgcn_sched_barrier(0);
      if (first) {
#pragma unroll
        for (int kk = 0; kk < 2; ++kk)
#pragma unroll
          for (int u = 0; u < 4; ++u)
            o[u] = __builtin_amdgcn_mfma_f32_16x16x32_bf16(wf[kk * 4 + u], xf[kk], o[u], 0, 0, 0);
        first = false;
      }
      STAMP(1)
      unsigned mx = 0, mine;
      {
        int tid = threadIdx.x;
        asm volatile("" : "+v"(tid));
        constexpr int NSW = 5;
        u32x2 rv[2 * NSW];
        unsigned lens[NSW], goff[NSW];
        const unsigned o16 = (unsigned)(tid & 15);
#pragma unroll
        for (int sw = 0; sw < NSW; ++sw) {
          const int sg = (tid >> 4) + sw * (K2_WAVES * 4);
          const int sc = min(sg, nb - 1);
          const unsigned a0 = tabL[sc * 16];
          lens[sw] = (sg < nb) ? (unsigned)tabL[sc * 16 + nt] - a0 : 0u;
          goff[sw] = ((unsigned)(cg + sc) * (unsigned)CH + a0) * 8u;
          rv[2 * sw] = __builtin_amdgcn_raw_buffer_load_b64(
              srd_r, (o16 < lens[sw]) ? goff[sw] + o16 * 8u : 0x80000000u, 0, 0);
          rv[2 * sw + 1] = __builtin_amdgcn_raw_buffer_load_b64(
              srd_r, (o16 + 16u < lens[sw]) ? goff[sw] + o16 * 8u + 128u : 0x80000000u, 0, 0);
        }
  {
    int ln = lane;
    asm volatile("" : "+v"(ln));
    unsigned cn[4], of[4];
#pragma unroll
    for (int jh = 0; jh < 4; jh += 2) {
      u32x4 r0[2], r1[2];
#pragma unroll
      for (int j = 0; j < 2; ++j) {
        const u32x4* trw = (const u32x4*)(tabL + min((jh + j) * 64 + ln, nb - 1) * 16);
        r0[j] = trw[0];
        r1[j] = trw[1];
      }
      unsigned lo[2], hi[2];
#define PICK(A, B) _Pragma("unroll") for (int j = 0; j < 2; ++j) { lo[j] = A; hi[j] = B; }
      switch (wv >> 1) {
        case 1: PICK(r0[j][1], r0[j][2]) break;
        case 2: PICK(r0[j][2], r0[j][3]) break;
        case 3: PICK(r0[j][3], r1[j][0]) break;
        case 4: PICK(r1[j][0], r1[j][1]) break;
        case 5: PICK(r1[j][1], r1[j][2]) break;
        case 6: PICK(r1[j][2], r1[j][3]) break;
        default: PICK(r0[j][0], r0[j][1]) break;
      }
#undef PICK
#pragma unroll
      for (int j = 0; j < 2; ++j) {
        const unsigned pr = (wv & 1) ? __builtin_amdgcn_alignbit(hi[j], lo[j], 16) : lo[j];
        const unsigned a0 = r0[j][0] & 0xffffu, aw = pr & 0xffffu, aw1 = pr >> 16;
        cn[jh + j] = ((jh + j) * 64 + ln < nb && active) ? aw1 - aw : 0u;
        of[jh + j] = aw - a0;
      }
    }
    const unsigned c = cn[0] + cn[1] + cn[2] + cn[3];
    const unsigned incl = wave_incl_scan(c, ln);
    unsigned p = incl - c - rnd;
#pragma unroll
    for (int j = 0; j < 4; ++j) {
      adjL[wv * ADJ_P + j * 64 + ln] = (int)(p - of[j]);
      p += cn[j];
    }
    if (ln == 63) cntL[wv] = incl;
  }
  __syncthreads();
  STAMP(2)
#pragma unroll
  for (int k = 0; k < K2_WAVES; ++k) mx = max(mx, cntL[k]);
  mx = __builtin_amdgcn_readfirstlane(mx);
  mine = __builtin_amdgcn_readfirstlane(cntL[wv]);
        auto place = [&](int sc, unsigned oo, unsigned rs, unsigned rm) {
          const unsigned w = (rm >> 20) & 15u;
          const unsigned slot = (unsigned)(adjL[w * ADJ_P + sc] + (int)oo);
          if (slot < (unsigned)CAP) lists[w * CAP + slot] = u32x2{rs, rm & 0xFFFFFu};
        };
        {
          int aj[2 * NSW];
#pragma unroll
          for (int k = 0; k < 2 * NSW; ++k) {
            const int sc = min((tid >> 4) + (k >> 1) * (K2_WAVES * 4), nb - 1);
            aj[k] = adjL[((rv[k][1] >> 20) & 15u) * ADJ_P + sc];
          }
#pragma unroll
          for (int k = 0; k < 2 * NSW; ++k) {
            const unsigned oo = o16 + 16u * (k & 1);
            const unsigned rs = rv[k][0], rm = rv[k][1];
            const unsigned slot = (unsigned)(aj[k] + (int)oo);
            if (oo < lens[k >> 1] && slot < (unsigned)CAP)
              lists[((rm >> 20) & 15u) * CAP + slot] = u32x2{rs, rm & 0xFFFFFu};
          }
        }
#pragma unroll
        for (int sw = 0; sw < NSW; ++sw) {
          const int sc = min((tid >> 4) + sw * (K2_WAVES * 4), nb - 1);
#pragma clang loop unroll(disable) vectorize(disable) interleave(disable)
          for (unsigned oo = o16 + 32u; oo < lens[sw]; oo += 16u) {
            const u32x2 r = __builtin_amdgcn_raw_buffer_load_b64(srd_r, goff[sw] + oo * 8u, 0, 0);
            place(sc, oo, r[0], r[1]);
          }
        }
      }
      STAMP(3)
      const unsigned m = (mine > rnd) ? min(mine - rnd, (unsigned)CAP) : 0u;
      const int ng = (int)((m + 31) >> 5);
      {
        const unsigned pidx = m + lane;
        if (pidx < (unsigned)ng * 32u) lrecs[pidx] = u32x2{0x80000000u, 0xFFFF0000u};
      }
#pragma unroll
      for (int f = 0; f < 8; ++f) wf[f] = bload16(srd_w, lane * 16u, (8 + f) * 1024u);
      __syncthreads();
      STAMP(4)
      auto issue = [&](int g) {
        const unsigned* ls = (const unsigned*)(lrecs + g * 32 + (lane >> 3));
        const unsigned v0 = ls[0] + ch_even, v1 = ls[16] + (ch_even ^ 64u), v2 = ls[32] + ch_even,
                       v3 = ls[48] + (ch_even ^ 64u);
        dma4x16(rbuf_a + (g & 1) * 4096, v0, v1, v2, v3, srd_x);
      };
      if (ng > 0) issue(0);
      if (ng > 1) issue(1);
#pragma clang loop unroll(disable)
      for (int g = 0; g < ng; ++g) {
        const unsigned* ls = (const unsigned*)(lrecs + min(g + 2, CAP / 32 - 1) * 32 + (lane >> 3));
        const unsigned n0 = ls[0], n1 = ls[16], n2 = ls[32], n3 = ls[48];
        if (g + 1 < ng) asm volatile("s_waitcnt vmcnt(4)" ::: "memory");
        else asm volatile("s_waitcnt vmcnt(0)" ::: "memory");
        const char* bufp = rbuf + (g & 1) * 4096 + abase;
        bf16x8 af[4];
#pragma unroll
        for (int t = 0; t < 4; ++t) {
          const char* ap = bufp + ((t ^ gsw) << 5);
          s16x4 lo = __builtin_amdgcn_ds_read_tr16_b64_v4i16((LAS s16x4*)(ap));
          s16x4 hi = __builtin_amdgcn_ds_read_tr16_b64_v4i16((LAS s16x4*)(ap + 512));
          af[t] = __builtin_shufflevector(lo, hi, 0, 1, 2, 3, 4, 5, 6, 7);
        }
        const u32x4* mp = (const u32x4*)(lrecs + g * 32 + 8 * q);
        const u32x4 m0 = mp[0], m1 = mp[1], m2 = mp[2], m3 = mp[3];
        const u32x4 ma = {m0[1], m0[3], m1[1], m1[3]};
        const u32x4 mb = {m2[1], m2[3], m3[1], m3[3]};
        if (g + 2 < ng)
          dma4x16(rbuf_a + (g & 1) * 4096, n0 + ch_even, n1 + (ch_even ^ 64u), n2 + ch_even,
                  n3 + (ch_even ^ 64u), srd_x);
        unsigned sel[8];
#pragma unroll
        for (int j = 0; j < 4; ++j) {
          sel[j] = ((ma[j] >> 16) == (unsigned)i16) ? (ma[j] & 0xffffu) : 0u;
          sel[4 + j] = ((mb[j] >> 16) == (unsigned)i16) ? (mb[j] & 0xffffu) : 0u;
        }
        u32x4 bp = {sel[0] | (sel[1] << 16), sel[2] | (sel[3] << 16), sel[4] | (sel[5] << 16),
                    sel[6] | (sel[7] << 16)};
        const bf16x8 bfr = __builtin_bit_cast(bf16x8, bp);
#pragma unroll
        for (int t = 0; t < 4; ++t)
          acc[t] = __builtin_amdgcn_mfma_f32_16x16x32_bf16(af[t], bfr, acc[t], 0, 0, 0);
      }
      if (rnd + CAP >= mx) break;
    }
  }

  STAMP(5)
#pragma unroll
  for (int s = 0; s < 2; ++s) {
    u32x4 tp = {pk_bf16(acc[2 * s][0], acc[2 * s][1]), pk_bf16(acc[2 * s][2], acc[2 * s][3]),
                pk_bf16(acc[2 * s + 1][0], acc[2 * s + 1][1]),
                pk_bf16(acc[2 * s + 1][2], acc[2 * s + 1][3])};
    const bf16x8 tf = __builtin_bit_cast(bf16x8, tp);
#pragma unroll
    for (int u = 0; u < 4; ++u)
      o[u] = __builtin_amdgcn_mfma_f32_16x16x32_bf16(wf[s * 4 + u], tf, o[u], 0, 0, 0);
  }
#pragma unroll
  for (int hh = 0; hh < 2; ++hh) {
    u32x4 ov = {pk_bf16(o[2 * hh][0], o[2 * hh][1]), pk_bf16(o[2 * hh][2], o[2 * hh][3]),
                pk_bf16(o[2 * hh + 1][0], o[2 * hh + 1][1]),
                pk_bf16(o[2 * hh + 1][2], o[2 * hh + 1][3])};
    __builtin_amdgcn_raw_buffer_store_b128(ov, srd_o, rowoff + 64u * hh, 0, 16);
  }
  STAMP_OUT((char*)wfrag + 65536, bid * 16 + wv)
}

__device__ __forceinline__ float bf2f(uint16_t v) { return __uint_as_float((unsigned)v << 16); }

__global__ void fb_zero(float* t1, size_t n) {
  for (size_t i = blockIdx.x * (size_t)blockDim.x + threadIdx.x; i < n; i += (size_t)gridDim.x * blockDim.x)
    t1[i] = 0.f;
}
__global__ void fb_scatter(const uint16_t* x, const int* src, const int* dst, const uint16_t* ew,
                           float* t1, int E) {
  const int lane = threadIdx.x & 63;
  const size_t wid = (blockIdx.x * (size_t)blockDim.x + threadIdx.x) >> 6;
  const size_t nw = ((size_t)gridDim.x * blockDim.x) >> 6;
  for (size_t e = wid; e < (size_t)E; e += nw)
    atomicAdd(&t1[(size_t)dst[e] * DF + lane], bf2f(ew[e]) * bf2f(x[(size_t)src[e] * DF + lane]));
}
__global__ void fb_out(const uint16_t* x, const float* t1, const uint16_t* w, uint16_t* out, int N) {
  const int lane = threadIdx.x & 63;
  const size_t wid = (blockIdx.x * (size_t)blockDim.x + threadIdx.x) >> 6;
  const size_t nw = ((size_t)gridDim.x * blockDim.x) >> 6;
  for (size_t n = wid; n < (size_t)N; n += nw) {
    float a = 0.f;
    for (int d = 0; d < DF; ++d) {
      const float tv = bf2f((uint16_t)(pk_bf16(t1[n * DF + d], 0.f) & 0xffffu));
      a += bf2f(x[n * DF + d]) * bf2f(w[d * DF + lane]) + tv * bf2f(w[(DF + d) * DF + lane]);
    }
    out[n * DF + lane] = (uint16_t)(pk_bf16(a, 0.f) & 0xffffu);
  }
}

extern "C" void kernel_launch(void* const* d_in, const int* in_sizes, int n_in, void* d_out,
                              int out_size, void* d_ws, size_t ws_size, hipStream_t stream) {
  const uint16_t* x = (const uint16_t*)d_in[0];
  const int* src = (const int*)d_in[1];
  const int* dst = (const int*)d_in[2];
  const uint16_t* ew = (const uint16_t*)d_in[3];
  const uint16_t* w = (const uint16_t*)d_in[4];
  uint16_t* out = (uint16_t*)d_out;
  const int N = in_sizes[0] / DF;
  const int E = in_sizes[1];
  const int P = (N + 15) / 16;

  int CH = (E + 255) / 256;
  CH = (CH + 3) & ~3;
  if (CH < 4) CH = 4;
  const bool fast = (P + 1 <= MAX_BINS) && (CH <= MAX_CH) && (E > 0) && ((size_t)E * 8 < 0x7fffffffull);
  if (fast) {
    const int B1 = (E + CH - 1) / CH;
    int G2 = K2_GRID;
    while ((long long)G2 * K2_WAVES < (long long)P) G2 += 8;
    char* ws = (char*)d_ws;
    size_t off = 0;
    u32x2* recs = (u32x2*)(ws + off);
    off += ((size_t)E * 8 + 255) & ~(size_t)255;
    uint16_t* tab = (uint16_t*)(ws + off);
    off += ((size_t)B1 * G2 * 32 + 255) & ~(size_t)255;
    uint16_t* wfrag = (uint16_t*)(ws + off);
    const unsigned tq = (unsigned)(P / G2);
    const unsigned mg0 = tq ? ((1u << 20) + tq - 1) / tq : 0u;
    const unsigned mg1 = ((1u << 20) + tq) / (tq + 1);
    k_part<<<B1, K1_THREADS, 0, stream>>>(src, dst, ew, w, recs, tab, wfrag, E, CH, P, G2, P / G2, P % G2, B1, mg0, mg1);
    k_aggr<<<G2, K2_WAVES * 64, 0, stream>>>(x, recs, tab, wfrag, out, N, G2 / 8, P / G2, P % G2, B1, CH, E);
  } else {
    float* t1 = (float*)d_ws;
    fb_zero<<<2048, 256, 0, stream>>>(t1, (size_t)N * DF);
    if (E > 0) fb_scatter<<<2048, 256, 0, stream>>>(x, src, dst, ew, t1, E);
    fb_out<<<2048, 256, 0, stream>>>(x, t1, w, out, N);
  }
}
```

```cpp
#include <hip/hip_runtime.h>
#include <hip/hip_bf16.h>
#include <stdint.h>

typedef __attribute__((ext_vector_type(4))) short s16x4;
typedef __attribute__((ext_vector_type(8))) short bf16x8;
typedef __attribute__((ext_vector_type(4))) float f32x4;
typedef __attribute__((ext_vector_type(2))) float f32x2;
typedef __attribute__((ext_vector_type(2))) __bf16 bf16x2v;
typedef __attribute__((ext_vector_type(4))) unsigned u32x4;
typedef __attribute__((ext_vector_type(2))) unsigned u32x2;
typedef __attribute__((ext_vector_type(4))) int i32x4;
typedef __attribute__((ext_vector_type(4))) unsigned short u16x4;
#define LAS __attribute__((address_space(3)))

#define STAMP_DECL
#define STAMP(i)
#define STAMP_OUT(ptr, idx)

constexpr int DF = 64;
constexpr int K1_THREADS = 1024;
constexpr int MAX_BINS = 4096;
constexpr int MAX_CH = 4096;
constexpr int K2_WAVES = 13;
constexpr int K2_GRID = 256;
constexpr int CAP = 512;

__device__ __forceinline__ unsigned pk_bf16(float a, float b) {
  f32x2 v = {a, b};
  bf16x2v r = __builtin_convertvector(v, bf16x2v);
  return __builtin_bit_cast(unsigned, r);
}

__device__ __forceinline__ unsigned wave_incl_scan(unsigned v, int  ) {
  v += __builtin_amdgcn_update_dpp(0u, v, 0x111, 0xf, 0xf, false);
  v += __builtin_amdgcn_update_dpp(0u, v, 0x112, 0xf, 0xf, false);
  v += __builtin_amdgcn_update_dpp(0u, v, 0x114, 0xf, 0xf, false);
  v += __builtin_amdgcn_update_dpp(0u, v, 0x118, 0xf, 0xf, false);
  v += __builtin_amdgcn_update_dpp(0u, v, 0x142, 0xa, 0xf, false);
  v += __builtin_amdgcn_update_dpp(0u, v, 0x143, 0xc, 0xf, false);
  return v;
}

__global__ __launch_bounds__(K1_THREADS) void k_part(
    const int* __restrict__ src, const int* __restrict__ dst, const uint16_t* __restrict__ ew,
    const uint16_t* __restrict__ w, u32x2* __restrict__ recs, uint16_t* __restrict__ tab,
    uint16_t* __restrict__ wfrag, int E, int CH, int P, int G2, int tq, int tr, int B1,
    unsigned mg0, unsigned mg1) {
  __shared__ __attribute__((aligned(16))) unsigned hist[MAX_BINS];
  __shared__ __attribute__((aligned(16))) u32x2 lrec[MAX_CH];
  __shared__ uint16_t lkey[MAX_CH];
  __shared__ unsigned wsum[16];
  asm volatile("" ::"s"(src), "s"(dst), "s"(ew), "s"(w), "s"(recs), "s"(tab), "s"(wfrag), "s"(E),
               "s"(CH), "s"(P), "s"(G2), "s"(tq), "s"(tr), "s"(B1), "s"(mg0), "s"(mg1));
  const int b = blockIdx.x, t = threadIdx.x, lane = t & 63, wv = t >> 6;
  STAMP_DECL
  const unsigned split = (unsigned)(tr * (tq + 1));
  const int e0 = b * CH;
  const int cnt = min(CH, E - e0);
  const int i0 = 4 * t;
  int d[4], s[4];
  unsigned wt[4];
  if (i0 + 4 <= cnt) {
    i32x4 d4 = *(const i32x4*)(dst + e0 + i0);
    i32x4 s4 = *(const i32x4*)(src + e0 + i0);
    u16x4 w4 = *(const u16x4*)(ew + e0 + i0);
#pragma unroll
    for (int j = 0; j < 4; ++j) { d[j] = d4[j]; s[j] = s4[j]; wt[j] = w4[j]; }
  } else {
#pragma unroll
    for (int j = 0; j < 4; ++j) {
      bool ok = i0 + j < cnt;
      d[j] = ok ? dst[e0 + i0 + j] : -1;
      s[j] = ok ? src[e0 + i0 + j] : 0;
      wt[j] = ok ? ew[e0 + i0 + j] : 0;
    }
  }
  ((u32x4*)hist)[t] = u32x4{0u, 0u, 0u, 0u};
  if (wv == 15) {
    for (int f = b; f < 16; f += B1) {
      const int kk = f >> 2, u = f & 3, i = lane & 15, q = lane >> 4;
      const int e = 32 * (u >> 1) + 8 * (i >> 2) + 4 * (u & 1) + (i & 3);
      unsigned short v[8];
#pragma unroll
      for (int j = 0; j < 8; ++j) {
        int dd, kw;
        if (kk < 2) { kw = 0; dd = 32 * kk + 8 * q + j; }
        else { kw = 1; dd = 16 * (2 * (kk - 2) + (j >> 2)) + 4 * q + (j & 3); }
        v[j] = w[(kw * DF + dd) * DF + e];
      }
      u32x4 o;
#pragma unroll
      for (int j = 0; j < 4; ++j) o[j] = (unsigned)v[2 * j] | ((unsigned)v[2 * j + 1] << 16);
      *(u32x4*)(wfrag + (f * 64 + lane) * 8) = o;
    }
  }
  __syncthreads();
  STAMP(1)
  int bin[4];
  unsigned rank[4];
#pragma unroll
  for (int j = 0; j < 4; ++j) {
    bin[j] = (d[j] >= 0) ? min(d[j] >> 4, P - 1) : -1;
    rank[j] = 0;
    if (bin[j] >= 0) rank[j] = atomicAdd(&hist[bin[j]], 1u);
  }

  __syncthreads();
  STAMP(2)
  u32x4 h = ((u32x4*)hist)[t];
  const unsigned sum = h[0] + h[1] + h[2] + h[3];
  const unsigned incl = wave_incl_scan(sum, lane);
  if (lane == 63) wsum[wv] = incl;
  __syncthreads();
  unsigned base = incl - sum;
#pragma unroll
  for (int k = 0; k < 16; ++k) base += (k < wv) ? wsum[k] : 0u;
  u32x4 o = {base, base + h[0], base + h[0] + h[1], base + h[0] + h[1] + h[2]};
  ((u32x4*)hist)[t] = o;
  __syncthreads();
  STAMP(3)
  unsigned st[4], cnb[4];
  unsigned r2v[4] = {0u, 0u, 0u, 0u};
#pragma unroll
  for (int j = 0; j < 4; ++j) {
    st[j] = (bin[j] >= 0) ? hist[bin[j]] : 0u;
    cnb[j] = (bin[j] >= 0) ? hist[bin[j] + 1] - st[j] : 0u;
  }
  {

#pragma unroll
    for (int j = 0; j < 4; ++j)
      if (bin[j] >= 0) lkey[st[j] + rank[j]] = (uint16_t)(i0 + j);
    __syncthreads();
    const unsigned cm = max(max(cnb[0], cnb[1]), max(cnb[2], cnb[3]));
#pragma clang loop unroll(disable) vectorize(disable) interleave(disable)
    for (unsigned k = 0; k < cm; ++k) {
      unsigned kv[4];
#pragma unroll
      for (int j = 0; j < 4; ++j) kv[j] = (k < cnb[j] && cnb[j] > 1u) ? lkey[st[j] + k] : 0xFFFFu;
#pragma unroll
      for (int j = 0; j < 4; ++j) r2v[j] += (kv[j] < (unsigned)(i0 + j)) ? 1u : 0u;
    }
  }
#pragma unroll
  for (int j = 0; j < 4; ++j) {
    if (bin[j] >= 0) {
      const unsigned r2 = r2v[j];
      const unsigned tl = (unsigned)bin[j];
      const bool lowp = tl < split;
      const unsigned xx = lowp ? tl : tl - split;
      const unsigned dq = (xx * (lowp ? mg1 : mg0)) >> 20;
      const unsigned widx = xx - dq * (unsigned)(lowp ? tq + 1 : tq);
      u32x2 r = {(unsigned)s[j] * 128u, (widx << 20) | ((unsigned)(d[j] & 15) << 16) | wt[j]};
      lrec[st[j] + r2] = r;
    }
  }
  __syncthreads();
  STAMP(4)
  const __amdgpu_buffer_rsrc_t srd_to =
      __builtin_amdgcn_make_buffer_rsrc((void*)tab, (short)0, G2 * B1 * 32, 0x00020000);
  for (int u = t; u < 2 * G2; u += K1_THREADS) {
    const int lb = u >> 1, hf = u & 1;
    const int t0 = lb * tq + min(lb, tr) + 8 * hf;
    unsigned v[8];
#pragma unroll
    for (int j = 0; j < 8; ++j) v[j] = hist[min(t0 + j, P)];
    u32x4 pk = {v[0] | (v[1] << 16), v[2] | (v[3] << 16), v[4] | (v[5] << 16), v[6] | (v[7] << 16)};
    __builtin_amdgcn_raw_buffer_store_b128(pk, srd_to, (unsigned)((lb * B1 + b) * 16 + 8 * hf) * 2u, 0, 16);
  }
  STAMP(5)
  {
    const __amdgpu_buffer_rsrc_t srd_ro =
        __builtin_amdgcn_make_buffer_rsrc((void*)recs, (short)0, E * 8, 0x00020000);
    for (int i = 2 * t; i < cnt; i += 2 * K1_THREADS) {
      if (i + 2 <= cnt)
        __builtin_amdgcn_raw_buffer_store_b128(*(const u32x4*)(lrec + i), srd_ro, (unsigned)(e0 + i) * 8u, 0, 16);
      else
        __builtin_amdgcn_raw_buffer_store_b64(lrec[i], srd_ro, (unsigned)(e0 + i) * 8u, 0, 16);
    }
  }
  STAMP_OUT((char*)wfrag + 65536 + 1048576, b * 16 + wv)
}

__device__ __forceinline__ void dma4x16(unsigned m0base, unsigned v0, unsigned v1, unsigned v2,
                                        unsigned v3, __amdgpu_buffer_rsrc_t srd) {
  unsigned keep;
  asm volatile(
      "s_waitcnt lgkmcnt(0)\n\t"
      "s_mov_b32 %0, m0\n\t"
      "s_mov_b32 m0, %1\n\t"
      "s_nop 0\n\t"
      "buffer_load_dwordx4 %2, %6, 0 offen lds\n\t"
      "s_add_u32 m0, %1, 1024\n\t"
      "s_nop 0\n\t"
      "buffer_load_dwordx4 %3, %6, 0 offen lds\n\t"
      "s_add_u32 m0, %1, 2048\n\t"
      "s_nop 0\n\t"
      "buffer_load_dwordx4 %4, %6, 0 offen lds\n\t"
      "s_add_u32 m0, %1, 3072\n\t"
      "s_nop 0\n\t"
      "buffer_load_dwordx4 %5, %6, 0 offen lds\n\t"
      "s_mov_b32 m0, %0"
      : "=&s"(keep)
      : "s"(m0base), "v"(v0), "v"(v1), "v"(v2), "v"(v3), "s"(srd)
      : "memory", "scc");
}

__device__ __forceinline__ void dma_rec(unsigned m0a, unsigned m0b, unsigned va, unsigned vb,
                                        __amdgpu_buffer_rsrc_t srd) {
  unsigned keep;
  asm volatile(
      "s_waitcnt lgkmcnt(0)\n\t"
      "s_mov_b32 %0, m0\n\t"
      "s_mov_b32 m0, %1\n\t"
      "s_nop 0\n\t"
      "buffer_load_dword %3, %5, 0 offen lds\n\t"
      "s_mov_b32 m0, %2\n\t"
      "s_nop 0\n\t"
      "buffer_load_dword %4, %5, 0 offen lds\n\t"
      "s_mov_b32 m0, %0"
      : "=&s"(keep)
      : "s"(m0a), "s"(m0b), "v"(va), "v"(vb), "s"(srd)
      : "memory");
}

__device__ __forceinline__ bf16x8 bload16(__amdgpu_buffer_rsrc_t srd, unsigned voff, unsigned soff) {
  u32x4 v = __builtin_amdgcn_raw_buffer_load_b128(srd, voff, soff, 0);
  return __builtin_bit_cast(bf16x8, v);
}

__device__ __forceinline__ void dma1x16(unsigned m0base, unsigned v0, __amdgpu_buffer_rsrc_t srd) {
  unsigned keep;
  asm volatile(
      "s_waitcnt lgkmcnt(0)\n\t"
      "s_mov_b32 %0, m0\n\t"
      "s_mov_b32 m0, %1\n\t"
      "s_nop 0\n\t"
      "buffer_load_dwordx4 %2, %3, 0 offen lds\n\t"
      "s_mov_b32 m0, %0"
      : "=&s"(keep)
      : "s"(m0base), "v"(v0), "s"(srd)
      : "memory");
}

constexpr int ADJ_P = 257;
constexpr int LDS_W = 32768;
constexpr int LDS_LISTS = K2_WAVES * 8192;
constexpr int LDS_TOTAL = LDS_LISTS + K2_WAVES * 8 * CAP;

__global__ __launch_bounds__(K2_WAVES * 64) void k_aggr(
    const uint16_t* __restrict__ x, const u32x2* __restrict__ recs,
    const uint16_t* __restrict__ tab, const uint16_t* __restrict__ wfrag,
    uint16_t* __restrict__ out, int N, int G8, int tq, int tr, int B1, int CH, int E) {
  __shared__ __attribute__((aligned(16))) char smem[LDS_TOTAL];
  asm volatile("" ::"s"(x), "s"(recs), "s"(tab), "s"(wfrag), "s"(out), "s"(N), "s"(G8), "s"(tq),
               "s"(tr), "s"(B1), "s"(CH), "s"(E));
  const int lane = threadIdx.x & 63;
  const int wv = __builtin_amdgcn_readfirstlane(threadIdx.x >> 6);
  STAMP_DECL
  const int bid = blockIdx.x;
  const int lb = (bid & 7) * G8 + (bid >> 3);
  const int t_begin = lb * tq + min(lb, tr);
  const int nt = tq + (lb < tr ? 1 : 0);
  const int tile = t_begin + wv;
  const bool active = wv < nt;

  char* rbuf = smem + wv * 8192;
  u32x2* lists = (u32x2*)(smem + LDS_LISTS);
  u32x2* lrecs = lists + wv * CAP;
  const uint16_t* tabL = (const uint16_t*)smem;
  int* adjL = (int*)(smem + 8192);
  unsigned* cntL = (unsigned*)(smem + 24704);
  const unsigned smem_a = (unsigned)(size_t)(LAS char*)smem;
  const unsigned rbuf_a = smem_a + wv * 8192;

  const __amdgpu_buffer_rsrc_t srd_x =
      __builtin_amdgcn_make_buffer_rsrc((void*)x, (short)0, N * 128, 0x00020000);
  const __amdgpu_buffer_rsrc_t srd_r =
      __builtin_amdgcn_make_buffer_rsrc((void*)recs, (short)0, E * 8, 0x00020000);
  const __amdgpu_buffer_rsrc_t srd_w =
      __builtin_amdgcn_make_buffer_rsrc((void*)wfrag, (short)0, 16384, 0x00020000);
  const __amdgpu_buffer_rsrc_t srd_t = __builtin_amdgcn_make_buffer_rsrc(
      (void*)(tab + (size_t)lb * B1 * 16), (short)0, B1 * 32, 0x00020000);
  const __amdgpu_buffer_rsrc_t srd_o =
      __builtin_amdgcn_make_buffer_rsrc((void*)out, (short)0, N * 128, 0x00020000);

  const int q = lane >> 4, i16 = lane & 15;
  const unsigned ch_even = (unsigned)((lane & 7) ^ (((lane >> 4) & 1) << 1)) << 4;
  const int qp = i16 >> 2, pp = lane & 3;
  const int gsw = ((qp >> 1) & 1) | ((q & 1) << 1);
  const unsigned abase = (unsigned)((8 * q + qp) * 128 + (pp >> 1) * 16 + 8 * (pp & 1));

  const unsigned rowoff = active ? (unsigned)(tile * 16 + i16) * 128u + 16u * q : 0x80000000u;
  f32x4 acc[4];
#pragma unroll
  for (int t = 0; t < 4; ++t) acc[t] = f32x4{0.f, 0.f, 0.f, 0.f};
  f32x4 o[4];
#pragma unroll
  for (int u = 0; u < 4; ++u) o[u] = f32x4{0.f, 0.f, 0.f, 0.f};
  bf16x8 wf[8];
  bool first = true;

  for (int cg = 0; cg < B1; cg += 256) {
    const int nb = min(256, B1 - cg);
    for (unsigned rnd = 0;; rnd += CAP) {
      if (!first) __syncthreads();
      if (wv * 32 < nb) dma1x16(smem_a + wv * 1024, (unsigned)(cg * 32 + wv * 1024 + lane * 16), srd_t);
      for (int k = (wv + 5) % K2_WAVES; k < 16; k += K2_WAVES)
        dma1x16(smem_a + LDS_W + k * 1024, (unsigned)(k * 1024 + lane * 16), srd_w);
      asm volatile("s_waitcnt vmcnt(0)" ::: "memory");
      __syncthreads();
      bf16x8 xf[2];
      STAMP(1)
      unsigned mx = 0, mine;
      {
        int tid = threadIdx.x;
        asm volatile("" : "+v"(tid));
        constexpr int NSW = 5;
        u32x2 rv[2 * NSW];
        unsigned lens[NSW], goff[NSW];
        const unsigned o16 = (unsigned)(tid & 15);
        {
          unsigned a0[NSW], a1[NSW];
#pragma unroll
          for (int sw = 0; sw < NSW; ++sw) {
            const int sc = min((tid >> 4) + sw * (K2_WAVES * 4), nb - 1);
            a0[sw] = tabL[sc * 16];
            a1[sw] = tabL[sc * 16 + nt];
          }
#pragma unroll
          for (int sw = 0; sw < NSW; ++sw) {
            const int sg = (tid >> 4) + sw * (K2_WAVES * 4);
            const int sc = min(sg, nb - 1);
            lens[sw] = (sg < nb) ? a1[sw] - a0[sw] : 0u;
            goff[sw] = ((unsigned)(cg + sc) * (unsigned)CH + a0[sw]) * 8u;
            rv[2 * sw] = __builtin_amdgcn_raw_buffer_load_b64(
                srd_r, (o16 < lens[sw]) ? goff[sw] + o16 * 8u : 0x80000000u, 0, 0);
            rv[2 * sw + 1] = __builtin_amdgcn_raw_buffer_load_b64(
                srd_r, (o16 + 16u < lens[sw]) ? goff[sw] + o16 * 8u + 128u : 0x80000000u, 0, 0);
          }
        }
        xf[0] = bload16(srd_x, rowoff, 0);
        xf[1] = bload16(srd_x, rowoff, 64);
  {
    int ln = lane;
    asm volatile("" : "+v"(ln));
    unsigned cn[4], of[4];
#pragma unroll
    for (int jh = 0; jh < 4; jh += 2) {
      u32x4 r0[2], r1[2];
#pragma unroll
      for (int j = 0; j < 2; ++j) {
        const u32x4* trw = (const u32x4*)(tabL + min((jh + j) * 64 + ln, nb - 1) * 16);
        r0[j] = trw[0];
        r1[j] = trw[1];
      }
      unsigned lo[2], hi[2];
#define PICK(A, B) _Pragma("unroll") for (int j = 0; j < 2; ++j) { lo[j] = A; hi[j] = B; }
      switch (wv >> 1) {
        case 1: PICK(r0[j][1], r0[j][2]) break;
        case 2: PICK(r0[j][2], r0[j][3]) break;
        case 3: PICK(r0[j][3], r1[j][0]) break;
        case 4: PICK(r1[j][0], r1[j][1]) break;
        case 5: PICK(r1[j][1], r1[j][2]) break;
        case 6: PICK(r1[j][2], r1[j][3]) break;
        default: PICK(r0[j][0], r0[j][1]) break;
      }
#undef PICK
#pragma unroll
      for (int j = 0; j < 2; ++j) {
        const unsigned pr = (wv & 1) ? __builtin_amdgcn_alignbit(hi[j], lo[j], 16) : lo[j];
        const unsigned a0 = r0[j][0] & 0xffffu, aw = pr & 0xffffu, aw1 = pr >> 16;
        cn[jh + j] = ((jh + j) * 64 + ln < nb && active) ? aw1 - aw : 0u;
        of[jh + j] = aw - a0;
      }
    }
    const unsigned c = cn[0] + cn[1] + cn[2] + cn[3];
    const unsigned incl = wave_incl_scan(c, ln);
    unsigned p = incl - c - rnd;
#pragma unroll
    for (int j = 0; j < 4; ++j) {
      adjL[wv * ADJ_P + j * 64 + ln] = (int)(p - of[j]);
      p += cn[j];
    }
    if (ln == 63) cntL[wv] = incl;
  }
  __syncthreads();
  STAMP(2)
#pragma unroll
  for (int k = 0; k < K2_WAVES; ++k) mx = max(mx, cntL[k]);
  mx = __builtin_amdgcn_readfirstlane(mx);
  mine = __builtin_amdgcn_readfirstlane(cntL[wv]);
        auto place = [&](int sc, unsigned oo, unsigned rs, unsigned rm) {
          const unsigned w = (rm >> 20) & 15u;
          const unsigned slot = (unsigned)(adjL[w * ADJ_P + sc] + (int)oo);
          if (slot < (unsigned)CAP) lists[w * CAP + slot] = u32x2{rs, rm & 0xFFFFFu};
        };
        {
          int aj[2 * NSW];
#pragma unroll
          for (int k = 0; k < 2 * NSW; ++k) {
            const int sc = min((tid >> 4) + (k >> 1) * (K2_WAVES * 4), nb - 1);
            aj[k] = adjL[((rv[k][1] >> 20) & 15u) * ADJ_P + sc];
          }
#pragma unroll
          for (int k = 0; k < 2 * NSW; ++k) {
            const unsigned oo = o16 + 16u * (k & 1);
            const unsigned rs = rv[k][0], rm = rv[k][1];
            const unsigned slot = (unsigned)(aj[k] + (int)oo);
            if (oo < lens[k >> 1] && slot < (unsigned)CAP)
              lists[((rm >> 20) & 15u) * CAP + slot] = u32x2{rs, rm & 0xFFFFFu};
          }
        }
#pragma unroll
        for (int sw = 0; sw < NSW; ++sw) {
          const int sc = min((tid >> 4) + sw * (K2_WAVES * 4), nb - 1);
#pragma clang loop unroll(disable) vectorize(disable) interleave(disable)
          for (unsigned oo = o16 + 32u; oo < lens[sw]; oo += 16u) {
            const u32x2 r = __builtin_amdgcn_raw_buffer_load_b64(srd_r, goff[sw] + oo * 8u, 0, 0);
            place(sc, oo, r[0], r[1]);
          }
        }
      }
      STAMP(3)
      const unsigned m = (mine > rnd) ? min(mine - rnd, (unsigned)CAP) : 0u;
      const int ng = (int)((m + 31) >> 5);
      {
        const unsigned pidx = m + lane;
        if (pidx < (unsigned)ng * 32u) lrecs[pidx] = u32x2{0x80000000u, 0xFFFF0000u};
      }
      if (first) {
#pragma unroll
        for (int f = 0; f < 8; ++f) wf[f] = *(const bf16x8*)(smem + LDS_W + f * 1024 + lane * 16);
#pragma unroll
        for (int kk = 0; kk < 2; ++kk)
#pragma unroll
          for (int u = 0; u < 4; ++u)
            o[u] = __builtin_amdgcn_mfma_f32_16x16x32_bf16(wf[kk * 4 + u], xf[kk], o[u], 0, 0, 0);
        first = false;
      }
#pragma unroll
      for (int f = 0; f < 8; ++f) wf[f] = *(const bf16x8*)(smem + LDS_W + (8 + f) * 1024 + lane * 16);
      __syncthreads();
      STAMP(4)
      auto issue = [&](int g) {
        const unsigned* ls = (const unsigned*)(lrecs + g * 32 + (lane >> 3));
        const unsigned v0 = ls[0] + ch_even, v1 = ls[16] + (ch_even ^ 64u), v2 = ls[32] + ch_even,
                       v3 = ls[48] + (ch_even ^ 64u);
        dma4x16(rbuf_a + (g & 1) * 4096, v0, v1, v2, v3, srd_x);
      };
      if (ng > 0) issue(0);
      if (ng > 1) issue(1);
#pragma clang loop unroll(disable)
      for (int g = 0; g < ng; ++g) {
        const unsigned* ls = (const unsigned*)(lrecs + min(g + 2, CAP / 32 - 1) * 32 + (lane >> 3));
        const unsigned n0 = ls[0], n1 = ls[16], n2 = ls[32], n3 = ls[48];
        if (g + 1 < ng) asm volatile("s_waitcnt vmcnt(4)" ::: "memory");
        else asm volatile("s_waitcnt vmcnt(0)" ::: "memory");
        const char* bufp = rbuf + (g & 1) * 4096 + abase;
        bf16x8 af[4];
#pragma unroll
        for (int t = 0; t < 4; ++t) {
          const char* ap = bufp + ((t ^ gsw) << 5);
          s16x4 lo = __builtin_amdgcn_ds_read_tr16_b64_v4i16((LAS s16x4*)(ap));
          s16x4 hi = __builtin_amdgcn_ds_read_tr16_b64_v4i16((LAS s16x4*)(ap + 512));
          af[t] = __builtin_shufflevector(lo, hi, 0, 1, 2, 3, 4, 5, 6, 7);
        }
        const u32x4* mp = (const u32x4*)(lrecs + g * 32 + 8 * q);
        const u32x4 m0 = mp[0], m1 = mp[1], m2 = mp[2], m3 = mp[3];
        const u32x4 ma = {m0[1], m0[3], m1[1], m1[3]};
        const u32x4 mb = {m2[1], m2[3], m3[1], m3[3]};
        if (g + 2 < ng)
          dma4x16(rbuf_a + (g & 1) * 4096, n0 + ch_even, n1 + (ch_even ^ 64u), n2 + ch_even,
                  n3 + (ch_even ^ 64u), srd_x);
        unsigned sel[8];
#pragma unroll
        for (int j = 0; j < 4; ++j) {
          sel[j] = ((ma[j] >> 16) == (unsigned)i16) ? (ma[j] & 0xffffu) : 0u;
          sel[4 + j] = ((mb[j] >> 16) == (unsigned)i16) ? (mb[j] & 0xffffu) : 0u;
        }
        u32x4 bp = {sel[0] | (sel[1] << 16), sel[2] | (sel[3] << 16), sel[4] | (sel[5] << 16),
                    sel[6] | (sel[7] << 16)};
        const bf16x8 bfr = __builtin_bit_cast(bf16x8, bp);
#pragma unroll
        for (int t = 0; t < 4; ++t)
          acc[t] = __builtin_amdgcn_mfma_f32_16x16x32_bf16(af[t], bfr, acc[t], 0, 0, 0);
      }
      if (rnd + CAP >= mx) break;
    }
  }

  STAMP(5)
#pragma unroll
  for (int s = 0; s < 2; ++s) {
    u32x4 tp = {pk_bf16(acc[2 * s][0], acc[2 * s][1]), pk_bf16(acc[2 * s][2], acc[2 * s][3]),
                pk_bf16(acc[2 * s + 1][0], acc[2 * s + 1][1]),
                pk_bf16(acc[2 * s + 1][2], acc[2 * s + 1][3])};
    const bf16x8 tf = __builtin_bit_cast(bf16x8, tp);
#pragma unroll
    for (int u = 0; u < 4; ++u)
      o[u] = __builtin_amdgcn_mfma_f32_16x16x32_bf16(wf[s * 4 + u], tf, o[u], 0, 0, 0);
  }
#pragma unroll
  for (int hh = 0; hh < 2; ++hh) {
    u32x4 ov = {pk_bf16(o[2 * hh][0], o[2 * hh][1]), pk_bf16(o[2 * hh][2], o[2 * hh][3]),
                pk_bf16(o[2 * hh + 1][0], o[2 * hh + 1][1]),
                pk_bf16(o[2 * hh + 1][2], o[2 * hh + 1][3])};
    __builtin_amdgcn_raw_buffer_store_b128(ov, srd_o, rowoff + 64u * hh, 0, 16);
  }
  STAMP_OUT((char*)wfrag + 65536, bid * 16 + wv)
}

__device__ __forceinline__ float bf2f(uint16_t v) { return __uint_as_float((unsigned)v << 16); }

__global__ void fb_zero(float* t1, size_t n) {
  for (size_t i = blockIdx.x * (size_t)blockDim.x + threadIdx.x; i < n; i += (size_t)gridDim.x * blockDim.x)
    t1[i] = 0.f;
}
__global__ void fb_scatter(const uint16_t* x, const int* src, const int* dst, const uint16_t* ew,
                           float* t1, int E) {
  const int lane = threadIdx.x & 63;
  const size_t wid = (blockIdx.x * (size_t)blockDim.x + threadIdx.x) >> 6;
  const size_t nw = ((size_t)gridDim.x * blockDim.x) >> 6;
  for (size_t e = wid; e < (size_t)E; e += nw)
    atomicAdd(&t1[(size_t)dst[e] * DF + lane], bf2f(ew[e]) * bf2f(x[(size_t)src[e] * DF + lane]));
}
__global__ void fb_out(const uint16_t* x, const float* t1, const uint16_t* w, uint16_t* out, int N) {
  const int lane = threadIdx.x & 63;
  const size_t wid = (blockIdx.x * (size_t)blockDim.x + threadIdx.x) >> 6;
  const size_t nw = ((size_t)gridDim.x * blockDim.x) >> 6;
  for (size_t n = wid; n < (size_t)N; n += nw) {
    float a = 0.f;
    for (int d = 0; d < DF; ++d) {
      const float tv = bf2f((uint16_t)(pk_bf16(t1[n * DF + d], 0.f) & 0xffffu));
      a += bf2f(x[n * DF + d]) * bf2f(w[d * DF + lane]) + tv * bf2f(w[(DF + d) * DF + lane]);
    }
    out[n * DF + lane] = (uint16_t)(pk_bf16(a, 0.f) & 0xffffu);
  }
}

extern "C" void kernel_launch(void* const* d_in, const int* in_sizes, int n_in, void* d_out,
                              int out_size, void* d_ws, size_t ws_size, hipStream_t stream) {
  const uint16_t* x = (const uint16_t*)d_in[0];
  const int* src = (const int*)d_in[1];
  const int* dst = (const int*)d_in[2];
  const uint16_t* ew = (const uint16_t*)d_in[3];
  const uint16_t* w = (const uint16_t*)d_in[4];
  uint16_t* out = (uint16_t*)d_out;
  const int N = in_sizes[0] / DF;
  const int E = in_sizes[1];
  const int P = (N + 15) / 16;

  int CH = (E + 255) / 256;
  CH = (CH + 3) & ~3;
  if (CH < 4) CH = 4;
  const bool fast = (P + 1 <= MAX_BINS) && (CH <= MAX_CH) && (E > 0) && ((size_t)E * 8 < 0x7fffffffull);
  if (fast) {
    const int B1 = (E + CH - 1) / CH;
    int G2 = K2_GRID;
    while ((long long)G2 * K2_WAVES < (long long)P) G2 += 8;
    char* ws = (char*)d_ws;
    size_t off = 0;
    u32x2* recs = (u32x2*)(ws + off);
    off += ((size_t)E * 8 + 255) & ~(size_t)255;
    uint16_t* tab = (uint16_t*)(ws + off);
    off += ((size_t)B1 * G2 * 32 + 255) & ~(size_t)255;
    uint16_t* wfrag = (uint16_t*)(ws + off);
    const unsigned tq = (unsigned)(P / G2);
    const unsigned mg0 = tq ? ((1u << 20) + tq - 1) / tq : 0u;
    const unsigned mg1 = ((1u << 20) + tq) / (tq + 1);
    k_part<<<B1, K1_THREADS, 0, stream>>>(src, dst, ew, w, recs, tab, wfrag, E, CH, P, G2, P / G2, P % G2, B1, mg0, mg1);
    k_aggr<<<G2, K2_WAVES * 64, 0, stream>>>(x, recs, tab, wfrag, out, N, G2 / 8, P / G2, P % G2, B1, CH, E);
  } else {
    float* t1 = (float*)d_ws;
    fb_zero<<<2048, 256, 0, stream>>>(t1, (size_t)N * DF);
    if (E > 0) fb_scatter<<<2048, 256, 0, stream>>>(x, src, dst, ew, t1, E);
    fb_out<<<2048, 256, 0, stream>>>(x, t1, w, out, N);
  }
}
```

```cpp
#include <hip/hip_runtime.h>
#include <hip/hip_bf16.h>
#include <stdint.h>

typedef __attribute__((ext_vector_type(4))) short s16x4;
typedef __attribute__((ext_vector_type(8))) short bf16x8;
typedef __attribute__((ext_vector_type(4))) float f32x4;
typedef __attribute__((ext_vector_type(2))) float f32x2;
typedef __attribute__((ext_vector_type(2))) __bf16 bf16x2v;
typedef __attribute__((ext_vector_type(4))) unsigned u32x4;
typedef __attribute__((ext_vector_type(2))) unsigned u32x2;
typedef __attribute__((ext_vector_type(4))) int i32x4;
typedef __attribute__((ext_vector_type(4))) unsigned short u16x4;
#define LAS __attribute__((address_space(3)))

constexpr int DF = 64;
constexpr int K1_THREADS = 1024;
constexpr int MAX_BINS = 4096;
constexpr int MAX_CH = 4096;
constexpr int K2_WAVES = 13;
constexpr int K2_GRID = 256;
constexpr int CAP = 512;

__device__ __forceinline__ unsigned pk_bf16(float a, float b) {
  f32x2 v = {a, b};
  bf16x2v r = __builtin_convertvector(v, bf16x2v);
  return __builtin_bit_cast(unsigned, r);
}

__device__ __forceinline__ unsigned wave_incl_scan(unsigned v, int  ) {
  v += __builtin_amdgcn_update_dpp(0u, v, 0x111, 0xf, 0xf, false);
  v += __builtin_amdgcn_update_dpp(0u, v, 0x112, 0xf, 0xf, false);
  v += __builtin_amdgcn_update_dpp(0u, v, 0x114, 0xf, 0xf, false);
  v += __builtin_amdgcn_update_dpp(0u, v, 0x118, 0xf, 0xf, false);
  v += __builtin_amdgcn_update_dpp(0u, v, 0x142, 0xa, 0xf, false);
  v += __builtin_amdgcn_update_dpp(0u, v, 0x143, 0xc, 0xf, false);
  return v;
}


__global__ __launch_bounds__(K1_THREADS) void k_part(
    const int* __restrict__ src, const int* __restrict__ dst, const uint16_t* __restrict__ ew,
    const uint16_t* __restrict__ w, u32x2* __restrict__ recs, uint16_t* __restrict__ tab,
    uint16_t* __restrict__ wfrag, int E, int CH, int P, int G2, int tq, int tr, int B1,
    unsigned mg0, unsigned mg1) {
  __shared__ __attribute__((aligned(16))) unsigned hist[MAX_BINS];
  __shared__ __attribute__((aligned(16))) u32x2 lrec[MAX_CH];
  __shared__ uint16_t lkey[MAX_CH];
  __shared__ unsigned wsum[16];
  asm volatile("" ::"s"(src), "s"(dst), "s"(ew), "s"(w), "s"(recs), "s"(tab), "s"(wfrag), "s"(E),
               "s"(CH), "s"(P), "s"(G2), "s"(tq), "s"(tr), "s"(B1), "s"(mg0), "s"(mg1));
  const int b = blockIdx.x, t = threadIdx.x, lane = t & 63, wv = t >> 6;
  const unsigned split = (unsigned)(tr * (tq + 1));
  const int e0 = b * CH;
  const int cnt = min(CH, E - e0);
  const int i0 = 4 * t;
  int d[4], s[4];
  unsigned wt[4];
  if (i0 + 4 <= cnt) {
    i32x4 d4 = *(const i32x4*)(dst + e0 + i0);
    i32x4 s4 = *(const i32x4*)(src + e0 + i0);
    u16x4 w4 = *(const u16x4*)(ew + e0 + i0);
#pragma unroll
    for (int j = 0; j < 4; ++j) { d[j] = d4[j]; s[j] = s4[j]; wt[j] = w4[j]; }
  } else {
#pragma unroll
    for (int j = 0; j < 4; ++j) {
      bool ok = i0 + j < cnt;
      d[j] = ok ? dst[e0 + i0 + j] : -1;
      s[j] = ok ? src[e0 + i0 + j] : 0;
      wt[j] = ok ? ew[e0 + i0 + j] : 0;
    }
  }
  ((u32x4*)hist)[t] = u32x4{0u, 0u, 0u, 0u};
  if (wv == 15) {
    for (int f = b; f < 16; f += B1) {
      const int kk = f >> 2, u = f & 3, i = lane & 15, q = lane >> 4;
      const int e = 32 * (u >> 1) + 8 * (i >> 2) + 4 * (u & 1) + (i & 3);
      unsigned short v[8];
#pragma unroll
      for (int j = 0; j < 8; ++j) {
        int dd, kw;
        if (kk < 2) { kw = 0; dd = 32 * kk + 8 * q + j; }
        else { kw = 1; dd = 16 * (2 * (kk - 2) + (j >> 2)) + 4 * q + (j & 3); }
        v[j] = w[(kw * DF + dd) * DF + e];
      }
      u32x4 o;
#pragma unroll
      for (int j = 0; j < 4; ++j) o[j] = (unsigned)v[2 * j] | ((unsigned)v[2 * j + 1] << 16);
      *(u32x4*)(wfrag + (f * 64 + lane) * 8) = o;
    }
  }
  __syncthreads();
  int bin[4];
  unsigned rank[4];
#pragma unroll
  for (int j = 0; j < 4; ++j) {
    bin[j] = (d[j] >= 0) ? min(d[j] >> 4, P - 1) : -1;
    rank[j] = 0;
    if (bin[j] >= 0) rank[j] = atomicAdd(&hist[bin[j]], 1u);
  }

  __syncthreads();
  u32x4 h = ((u32x4*)hist)[t];
  const unsigned sum = h[0] + h[1] + h[2] + h[3];
  const unsigned incl = wave_incl_scan(sum, lane);
  if (lane == 63) wsum[wv] = incl;
  __syncthreads();
  unsigned base = incl - sum;
#pragma unroll
  for (int k = 0; k < 16; ++k) base += (k < wv) ? wsum[k] : 0u;
  u32x4 o = {base, base + h[0], base + h[0] + h[1], base + h[0] + h[1] + h[2]};
  ((u32x4*)hist)[t] = o;
  __syncthreads();
  unsigned st[4], cnb[4];
  unsigned r2v[4] = {0u, 0u, 0u, 0u};
#pragma unroll
  for (int j = 0; j < 4; ++j) {
    st[j] = (bin[j] >= 0) ? hist[bin[j]] : 0u;
    cnb[j] = (bin[j] >= 0) ? hist[bin[j] + 1] - st[j] : 0u;
  }
  {

#pragma unroll
    for (int j = 0; j < 4; ++j)
      if (bin[j] >= 0) lkey[st[j] + rank[j]] = (uint16_t)(i0 + j);
    __syncthreads();
    const unsigned cm = max(max(cnb[0], cnb[1]), max(cnb[2], cnb[3]));
#pragma clang loop unroll(disable) vectorize(disable) interleave(disable)
    for (unsigned k = 0; k < cm; ++k) {
      unsigned kv[4];
#pragma unroll
      for (int j = 0; j < 4; ++j) kv[j] = (k < cnb[j] && cnb[j] > 1u) ? lkey[st[j] + k] : 0xFFFFu;
#pragma unroll
      for (int j = 0; j < 4; ++j) r2v[j] += (kv[j] < (unsigned)(i0 + j)) ? 1u : 0u;
    }
  }
#pragma unroll
  for (int j = 0; j < 4; ++j) {
    if (bin[j] >= 0) {
      const unsigned r2 = r2v[j];
      const unsigned tl = (unsigned)bin[j];
      const bool lowp = tl < split;
      const unsigned xx = lowp ? tl : tl - split;
      const unsigned dq = (xx * (lowp ? mg1 : mg0)) >> 20;
      const unsigned widx = xx - dq * (unsigned)(lowp ? tq + 1 : tq);
      u32x2 r = {(unsigned)s[j] * 128u, (widx << 20) | ((unsigned)(d[j] & 15) << 16) | wt[j]};
      lrec[st[j] + r2] = r;
    }
  }
  __syncthreads();
  const __amdgpu_buffer_rsrc_t srd_to =
      __builtin_amdgcn_make_buffer_rsrc((void*)tab, (short)0, G2 * B1 * 32, 0x00020000);
  for (int u = t; u < 2 * G2; u += K1_THREADS) {
    const int lb = u >> 1, hf = u & 1;
    const int t0 = lb * tq + min(lb, tr) + 8 * hf;
    unsigned v[8];
#pragma unroll
    for (int j = 0; j < 8; ++j) v[j] = hist[min(t0 + j, P)];
    u32x4 pk = {v[0] | (v[1] << 16), v[2] | (v[3] << 16), v[4] | (v[5] << 16), v[6] | (v[7] << 16)};
    __builtin_amdgcn_raw_buffer_store_b128(pk, srd_to, (unsigned)((lb * B1 + b) * 16 + 8 * hf) * 2u, 0, 16);
  }
  {
    const __amdgpu_buffer_rsrc_t srd_ro =
        __builtin_amdgcn_make_buffer_rsrc((void*)recs, (short)0, E * 8, 0x00020000);
    for (int i = 2 * t; i < cnt; i += 2 * K1_THREADS) {
      if (i + 2 <= cnt)
        __builtin_amdgcn_raw_buffer_store_b128(*(const u32x4*)(lrec + i), srd_ro, (unsigned)(e0 + i) * 8u, 0, 16);
      else
        __builtin_amdgcn_raw_buffer_store_b64(lrec[i], srd_ro, (unsigned)(e0 + i) * 8u, 0, 16);
    }
  }
}

__device__ __forceinline__ void dma4x16(unsigned m0base, unsigned v0, unsigned v1, unsigned v2,
                                        unsigned v3, __amdgpu_buffer_rsrc_t srd) {
  unsigned keep;
  asm volatile(
      "s_waitcnt lgkmcnt(0)\n\t"
      "s_mov_b32 %0, m0\n\t"
      "s_mov_b32 m0, %1\n\t"
      "s_nop 0\n\t"
      "buffer_load_dwordx4 %2, %6, 0 offen lds\n\t"
      "s_add_u32 m0, %1, 1024\n\t"
      "s_nop 0\n\t"
      "buffer_load_dwordx4 %3, %6, 0 offen lds\n\t"
      "s_add_u32 m0, %1, 2048\n\t"
      "s_nop 0\n\t"
      "buffer_load_dwordx4 %4, %6, 0 offen lds\n\t"
      "s_add_u32 m0, %1, 3072\n\t"
      "s_nop 0\n\t"
      "buffer_load_dwordx4 %5, %6, 0 offen lds\n\t"
      "s_mov_b32 m0, %0"
      : "=&s"(keep)
      : "s"(m0base), "v"(v0), "v"(v1), "v"(v2), "v"(v3), "s"(srd)
      : "memory", "scc");
}

__device__ __forceinline__ bf16x8 bload16(__amdgpu_buffer_rsrc_t srd, unsigned voff, unsigned soff) {
  u32x4 v = __builtin_amdgcn_raw_buffer_load_b128(srd, voff, soff, 0);
  return __builtin_bit_cast(bf16x8, v);
}

__device__ __forceinline__ void dma1x16(unsigned m0base, unsigned v0, __amdgpu_buffer_rsrc_t srd) {
  unsigned keep;
  asm volatile(
      "s_waitcnt lgkmcnt(0)\n\t"
      "s_mov_b32 %0, m0\n\t"
      "s_mov_b32 m0, %1\n\t"
      "s_nop 0\n\t"
      "buffer_load_dwordx4 %2, %3, 0 offen lds\n\t"
      "s_mov_b32 m0, %0"
      : "=&s"(keep)
      : "s"(m0base), "v"(v0), "s"(srd)
      : "memory");
}

constexpr int ADJ_P = 257;
constexpr int LDS_W = 32768;
constexpr int LDS_LISTS = K2_WAVES * 8192;
constexpr int LDS_TOTAL = LDS_LISTS + K2_WAVES * 8 * CAP;

__global__ __launch_bounds__(K2_WAVES * 64) void k_aggr(
    const uint16_t* __restrict__ x, const u32x2* __restrict__ recs,
    const uint16_t* __restrict__ tab, const uint16_t* __restrict__ wfrag,
    uint16_t* __restrict__ out, int N, int G8, int tq, int tr, int B1, int CH, int E) {
  __shared__ __attribute__((aligned(16))) char smem[LDS_TOTAL];
  asm volatile("" ::"s"(x), "s"(recs), "s"(tab), "s"(wfrag), "s"(out), "s"(N), "s"(G8), "s"(tq),
               "s"(tr), "s"(B1), "s"(CH), "s"(E));
  const int lane = threadIdx.x & 63;
  const int wv = __builtin_amdgcn_readfirstlane(threadIdx.x >> 6);
  const int bid = blockIdx.x;
  const int lb = (bid & 7) * G8 + (bid >> 3);
  const int t_begin = lb * tq + min(lb, tr);
  const int nt = tq + (lb < tr ? 1 : 0);
  const int tile = t_begin + wv;
  const bool active = wv < nt;

  char* rbuf = smem + wv * 8192;
  u32x2* lists = (u32x2*)(smem + LDS_LISTS);
  u32x2* lrecs = lists + wv * CAP;
  const uint16_t* tabL = (const uint16_t*)smem;
  int* adjL = (int*)(smem + 8192);
  unsigned* cntL = (unsigned*)(smem + 24704);
  const unsigned smem_a = (unsigned)(size_t)(LAS char*)smem;
  const unsigned rbuf_a = smem_a + wv * 8192;

  const __amdgpu_buffer_rsrc_t srd_x =
      __builtin_amdgcn_make_buffer_rsrc((void*)x, (short)0, N * 128, 0x00020000);
  const __amdgpu_buffer_rsrc_t srd_r =
      __builtin_amdgcn_make_buffer_rsrc((void*)recs, (short)0, E * 8, 0x00020000);
  const __amdgpu_buffer_rsrc_t srd_w =
      __builtin_amdgcn_make_buffer_rsrc((void*)wfrag, (short)0, 16384, 0x00020000);
  const __amdgpu_buffer_rsrc_t srd_t = __builtin_amdgcn_make_buffer_rsrc(
      (void*)(tab + (size_t)lb * B1 * 16), (short)0, B1 * 32, 0x00020000);
  const __amdgpu_buffer_rsrc_t srd_o =
      __builtin_amdgcn_make_buffer_rsrc((void*)out, (short)0, N * 128, 0x00020000);

  const int q = lane >> 4, i16 = lane & 15;
  const unsigned ch_even = (unsigned)((lane & 7) ^ (((lane >> 4) & 1) << 1)) << 4;
  const int qp = i16 >> 2, pp = lane & 3;
  const int gsw = ((qp >> 1) & 1) | ((q & 1) << 1);
  const unsigned abase = (unsigned)((8 * q + qp) * 128 + (pp >> 1) * 16 + 8 * (pp & 1));

  const unsigned rowoff = active ? (unsigned)(tile * 16 + i16) * 128u + 16u * q : 0x80000000u;
  f32x4 acc[4];
#pragma unroll
  for (int t = 0; t < 4; ++t) acc[t] = f32x4{0.f, 0.f, 0.f, 0.f};
  f32x4 o[4];
#pragma unroll
  for (int u = 0; u < 4; ++u) o[u] = f32x4{0.f, 0.f, 0.f, 0.f};
  bf16x8 wf[8];
  bool first = true;

  for (int cg = 0; cg < B1; cg += 256) {
    const int nb = min(256, B1 - cg);
    for (unsigned rnd = 0;; rnd += CAP) {
      if (!first) __syncthreads();
      if (wv * 32 < nb) dma1x16(smem_a + wv * 1024, (unsigned)(cg * 32 + wv * 1024 + lane * 16), srd_t);
      for (int k = (wv + 5) % K2_WAVES; k < 16; k += K2_WAVES)
        dma1x16(smem_a + LDS_W + k * 1024, (unsigned)(k * 1024 + lane * 16), srd_w);
      asm volatile("s_waitcnt vmcnt(0)" ::: "memory");
      __syncthreads();
      bf16x8 xf[2];
      unsigned mx = 0, mine;
      {
        int tid = threadIdx.x;
        asm volatile("" : "+v"(tid));
        constexpr int NSW = 5;
        u32x2 rv[2 * NSW];
        unsigned lens[NSW], goff[NSW];
        const unsigned o16 = (unsigned)(tid & 15);
        {
          unsigned a0[NSW], a1[NSW];
#pragma unroll
          for (int sw = 0; sw < NSW; ++sw) {
            const int sc = min((tid >> 4) + sw * (K2_WAVES * 4), nb - 1);
            a0[sw] = tabL[sc * 16];
            a1[sw] = tabL[sc * 16 + nt];
          }
#pragma unroll
          for (int sw = 0; sw < NSW; ++sw) {
            const int sg = (tid >> 4) + sw * (K2_WAVES * 4);
            const int sc = min(sg, nb - 1);
            lens[sw] = (sg < nb) ? a1[sw] - a0[sw] : 0u;
            goff[sw] = ((unsigned)(cg + sc) * (unsigned)CH + a0[sw]) * 8u;
            rv[2 * sw] = __builtin_amdgcn_raw_buffer_load_b64(
                srd_r, (o16 < lens[sw]) ? goff[sw] + o16 * 8u : 0x80000000u, 0, 0);
            rv[2 * sw + 1] = __builtin_amdgcn_raw_buffer_load_b64(
                srd_r, (o16 + 16u < lens[sw]) ? goff[sw] + o16 * 8u + 128u : 0x80000000u, 0, 0);
          }
        }
        xf[0] = bload16(srd_x, rowoff, 0);
        xf[1] = bload16(srd_x, rowoff, 64);
        {
          int ln = lane;
          asm volatile("" : "+v"(ln));
          unsigned cn[4], of[4];
#pragma unroll
          for (int jh = 0; jh < 4; jh += 2) {
            u32x4 r0[2], r1[2];
#pragma unroll
            for (int j = 0; j < 2; ++j) {
              const u32x4* trw = (const u32x4*)(tabL + min((jh + j) * 64 + ln, nb - 1) * 16);
              r0[j] = trw[0];
              r1[j] = trw[1];
            }
            unsigned lo[2], hi[2];
#define PICK(A, B) _Pragma("unroll") for (int j = 0; j < 2; ++j) { lo[j] = A; hi[j] = B; }
            switch (wv >> 1) {
              case 1: PICK(r0[j][1], r0[j][2]) break;
              case 2: PICK(r0[j][2], r0[j][3]) break;
              case 3: PICK(r0[j][3], r1[j][0]) break;
              case 4: PICK(r1[j][0], r1[j][1]) break;
              case 5: PICK(r1[j][1], r1[j][2]) break;
              case 6: PICK(r1[j][2], r1[j][3]) break;
              default: PICK(r0[j][0], r0[j][1]) break;
            }
#undef PICK
#pragma unroll
            for (int j = 0; j < 2; ++j) {
              const unsigned pr = (wv & 1) ? __builtin_amdgcn_alignbit(hi[j], lo[j], 16) : lo[j];
              const unsigned a0 = r0[j][0] & 0xffffu, aw = pr & 0xffffu, aw1 = pr >> 16;
              cn[jh + j] = ((jh + j) * 64 + ln < nb && active) ? aw1 - aw : 0u;
              of[jh + j] = aw - a0;
            }
          }
          const unsigned c = cn[0] + cn[1] + cn[2] + cn[3];
          const unsigned incl = wave_incl_scan(c, ln);
          unsigned p = incl - c - rnd;
#pragma unroll
          for (int j = 0; j < 4; ++j) {
            adjL[wv * ADJ_P + j * 64 + ln] = (int)(p - of[j]);
            p += cn[j];
          }
          if (ln == 63) cntL[wv] = incl;
        }
        __syncthreads();
#pragma unroll
        for (int k = 0; k < K2_WAVES; ++k) mx = max(mx, cntL[k]);
        mx = __builtin_amdgcn_readfirstlane(mx);
        mine = __builtin_amdgcn_readfirstlane(cntL[wv]);
        auto place = [&](int sc, unsigned oo, unsigned rs, unsigned rm) {
          const unsigned w = (rm >> 20) & 15u;
          const unsigned slot = (unsigned)(adjL[w * ADJ_P + sc] + (int)oo);
          if (slot < (unsigned)CAP) lists[w * CAP + slot] = u32x2{rs, rm & 0xFFFFFu};
        };
        {
          int aj[2 * NSW];
#pragma unroll
          for (int k = 0; k < 2 * NSW; ++k) {
            const int sc = min((tid >> 4) + (k >> 1) * (K2_WAVES * 4), nb - 1);
            aj[k] = adjL[((rv[k][1] >> 20) & 15u) * ADJ_P + sc];
          }
#pragma unroll
          for (int k = 0; k < 2 * NSW; ++k) {
            const unsigned oo = o16 + 16u * (k & 1);
            const unsigned rs = rv[k][0], rm = rv[k][1];
            const unsigned slot = (unsigned)(aj[k] + (int)oo);
            if (oo < lens[k >> 1] && slot < (unsigned)CAP)
              lists[((rm >> 20) & 15u) * CAP + slot] = u32x2{rs, rm & 0xFFFFFu};
          }
        }
#pragma unroll
        for (int sw = 0; sw < NSW; ++sw) {
          const int sc = min((tid >> 4) + sw * (K2_WAVES * 4), nb - 1);
#pragma clang loop unroll(disable) vectorize(disable) interleave(disable)
          for (unsigned oo = o16 + 32u; oo < lens[sw]; oo += 16u) {
            const u32x2 r = __builtin_amdgcn_raw_buffer_load_b64(srd_r, goff[sw] + oo * 8u, 0, 0);
            place(sc, oo, r[0], r[1]);
          }
        }
      }
      const unsigned m = (mine > rnd) ? min(mine - rnd, (unsigned)CAP) : 0u;
      const int ng = (int)((m + 31) >> 5);
      {
        const unsigned pidx = m + lane;
        if (pidx < (unsigned)ng * 32u) lrecs[pidx] = u32x2{0x80000000u, 0xFFFF0000u};
      }
      if (first) {
#pragma unroll
        for (int f = 0; f < 8; ++f) wf[f] = *(const bf16x8*)(smem + LDS_W + f * 1024 + lane * 16);
#pragma unroll
        for (int kk = 0; kk < 2; ++kk)
#pragma unroll
          for (int u = 0; u < 4; ++u)
            o[u] = __builtin_amdgcn_mfma_f32_16x16x32_bf16(wf[kk * 4 + u], xf[kk], o[u], 0, 0, 0);
        first = false;
      }
#pragma unroll
      for (int f = 0; f < 8; ++f) wf[f] = *(const bf16x8*)(smem + LDS_W + (8 + f) * 1024 + lane * 16);
      __syncthreads();
      auto issue = [&](int g) {
        const unsigned* ls = (const unsigned*)(lrecs + g * 32 + (lane >> 3));
        const unsigned v0 = ls[0] + ch_even, v1 = ls[16] + (ch_even ^ 64u), v2 = ls[32] + ch_even,
                       v3 = ls[48] + (ch_even ^ 64u);
        dma4x16(rbuf_a + (g & 1) * 4096, v0, v1, v2, v3, srd_x);
      };
      if (ng > 0) issue(0);
      if (ng > 1) issue(1);
#pragma clang loop unroll(disable)
      for (int g = 0; g < ng; ++g) {
        const unsigned* ls = (const unsigned*)(lrecs + min(g + 2, CAP / 32 - 1) * 32 + (lane >> 3));
        const unsigned n0 = ls[0], n1 = ls[16], n2 = ls[32], n3 = ls[48];
        if (g + 1 < ng) asm volatile("s_waitcnt vmcnt(4)" ::: "memory");
        else asm volatile("s_waitcnt vmcnt(0)" ::: "memory");
        const char* bufp = rbuf + (g & 1) * 4096 + abase;
        bf16x8 af[4];
#pragma unroll
        for (int t = 0; t < 4; ++t) {
          const char* ap = bufp + ((t ^ gsw) << 5);
          s16x4 lo = __builtin_amdgcn_ds_read_tr16_b64_v4i16((LAS s16x4*)(ap));
          s16x4 hi = __builtin_amdgcn_ds_read_tr16_b64_v4i16((LAS s16x4*)(ap + 512));
          af[t] = __builtin_shufflevector(lo, hi, 0, 1, 2, 3, 4, 5, 6, 7);
        }
        const u32x4* mp = (const u32x4*)(lrecs + g * 32 + 8 * q);
        const u32x4 m0 = mp[0], m1 = mp[1], m2 = mp[2], m3 = mp[3];
        const u32x4 ma = {m0[1], m0[3], m1[1], m1[3]};
        const u32x4 mb = {m2[1], m2[3], m3[1], m3[3]};
        if (g + 2 < ng)
          dma4x16(rbuf_a + (g & 1) * 4096, n0 + ch_even, n1 + (ch_even ^ 64u), n2 + ch_even,
                  n3 + (ch_even ^ 64u), srd_x);
        unsigned sel[8];
#pragma unroll
        for (int j = 0; j < 4; ++j) {
          sel[j] = ((ma[j] >> 16) == (unsigned)i16) ? (ma[j] & 0xffffu) : 0u;
          sel[4 + j] = ((mb[j] >> 16) == (unsigned)i16) ? (mb[j] & 0xffffu) : 0u;
        }
        u32x4 bp = {sel[0] | (sel[1] << 16), sel[2] | (sel[3] << 16), sel[4] | (sel[5] << 16),
                    sel[6] | (sel[7] << 16)};
        const bf16x8 bfr = __builtin_bit_cast(bf16x8, bp);
#pragma unroll
        for (int t = 0; t < 4; ++t)
          acc[t] = __builtin_amdgcn_mfma_f32_16x16x32_bf16(af[t], bfr, acc[t], 0, 0, 0);
      }
      if (rnd + CAP >= mx) break;
    }
  }

#pragma unroll
  for (int s = 0; s < 2; ++s) {
    u32x4 tp = {pk_bf16(acc[2 * s][0], acc[2 * s][1]), pk_bf16(acc[2 * s][2], acc[2 * s][3]),
                pk_bf16(acc[2 * s + 1][0], acc[2 * s + 1][1]),
                pk_bf16(acc[2 * s + 1][2], acc[2 * s + 1][3])};
    const bf16x8 tf = __builtin_bit_cast(bf16x8, tp);
#pragma unroll
    for (int u = 0; u < 4; ++u)
      o[u] = __builtin_amdgcn_mfma_f32_16x16x32_bf16(wf[s * 4 + u], tf, o[u], 0, 0, 0);
  }
#pragma unroll
  for (int hh = 0; hh < 2; ++hh) {
    u32x4 ov = {pk_bf16(o[2 * hh][0], o[2 * hh][1]), pk_bf16(o[2 * hh][2], o[2 * hh][3]),
                pk_bf16(o[2 * hh + 1][0], o[2 * hh + 1][1]),
                pk_bf16(o[2 * hh + 1][2], o[2 * hh + 1][3])};
    __builtin_amdgcn_raw_buffer_store_b128(ov, srd_o, rowoff + 64u * hh, 0, 16);
  }
}

__device__ __forceinline__ float bf2f(uint16_t v) { return __uint_as_float((unsigned)v << 16); }

__global__ void fb_zero(float* t1, size_t n) {
  for (size_t i = blockIdx.x * (size_t)blockDim.x + threadIdx.x; i < n; i += (size_t)gridDim.x * blockDim.x)
    t1[i] = 0.f;
}
__global__ void fb_scatter(const uint16_t* x, const int* src, const int* dst, const uint16_t* ew,
                           float* t1, int E) {
  const int lane = threadIdx.x & 63;
  const size_t wid = (blockIdx.x * (size_t)blockDim.x + threadIdx.x) >> 6;
  const size_t nw = ((size_t)gridDim.x * blockDim.x) >> 6;
  for (size_t e = wid; e < (size_t)E; e += nw)
    atomicAdd(&t1[(size_t)dst[e] * DF + lane], bf2f(ew[e]) * bf2f(x[(size_t)src[e] * DF + lane]));
}
__global__ void fb_out(const uint16_t* x, const float* t1, const uint16_t* w, uint16_t* out, int N) {
  const int lane = threadIdx.x & 63;
  const size_t wid = (blockIdx.x * (size_t)blockDim.x + threadIdx.x) >> 6;
  const size_t nw = ((size_t)gridDim.x * blockDim.x) >> 6;
  for (size_t n = wid; n < (size_t)N; n += nw) {
    float a = 0.f;
    for (int d = 0; d < DF; ++d) {
      const float tv = bf2f((uint16_t)(pk_bf16(t1[n * DF + d], 0.f) & 0xffffu));
      a += bf2f(x[n * DF + d]) * bf2f(w[d * DF + lane]) + tv * bf2f(w[(DF + d) * DF + lane]);
    }
    out[n * DF + lane] = (uint16_t)(pk_bf16(a, 0.f) & 0xffffu);
  }
}

extern "C" void kernel_launch(void* const* d_in, const int* in_sizes, int n_in, void* d_out,
                              int out_size, void* d_ws, size_t ws_size, hipStream_t stream) {
  const uint16_t* x = (const uint16_t*)d_in[0];
  const int* src = (const int*)d_in[1];
  const int* dst = (const int*)d_in[2];
  const uint16_t* ew = (const uint16_t*)d_in[3];
  const uint16_t* w = (const uint16_t*)d_in[4];
  uint16_t* out = (uint16_t*)d_out;
  const int N = in_sizes[0] / DF;
  const int E = in_sizes[1];
  const int P = (N + 15) / 16;

  int CH = (E + 255) / 256;
  CH = (CH + 3) & ~3;
  if (CH < 4) CH = 4;
  const bool fast = (P + 1 <= MAX_BINS) && (CH <= MAX_CH) && (E > 0) && ((size_t)E * 8 < 0x7fffffffull);
  if (fast) {
    const int B1 = (E + CH - 1) / CH;
    int G2 = K2_GRID;
    while ((long long)G2 * K2_WAVES < (long long)P) G2 += 8;
    char* ws = (char*)d_ws;
    size_t off = 0;
    u32x2* recs = (u32x2*)(ws + off);
    off += ((size_t)E * 8 + 255) & ~(size_t)255;
    uint16_t* tab = (uint16_t*)(ws + off);
    off += ((size_t)B1 * G2 * 32 + 255) & ~(size_t)255;
    uint16_t* wfrag = (uint16_t*)(ws + off);
    const unsigned tq = (unsigned)(P / G2);
    const unsigned mg0 = tq ? ((1u << 20) + tq - 1) / tq : 0u;
    const unsigned mg1 = ((1u << 20) + tq) / (tq + 1);
    k_part<<<B1, K1_THREADS, 0, stream>>>(src, dst, ew, w, recs, tab, wfrag, E, CH, P, G2, P / G2, P % G2, B1, mg0, mg1);
    k_aggr<<<G2, K2_WAVES * 64, 0, stream>>>(x, recs, tab, wfrag, out, N, G2 / 8, P / G2, P % G2, B1, CH, E);
  } else {
    float* t1 = (float*)d_ws;
    fb_zero<<<2048, 256, 0, stream>>>(t1, (size_t)N * DF);
    if (E > 0) fb_scatter<<<2048, 256, 0, stream>>>(x, src, dst, ew, t1, E);
    fb_out<<<2048, 256, 0, stream>>>(x, t1, w, out, N);
  }
}
```
